```python
import torch

def _softmax(x, axis=-1):
    return torch.softmax(x, dim=axis)

def _gelu(x):
    return torch.nn.functional.gelu(x, approximate='tanh')

def _silu(x):
    return torch.nn.functional.silu(x)

def _sigmoid(x):
    return torch.sigmoid(x)

def _layernorm(x, axis=-1, eps=1e-05):
    m = x.mean(dim=axis, keepdim=True)
    v = x.var(dim=axis, keepdim=True, unbiased=False)
    return (x - m) / torch.sqrt(v + eps)

def _scatter_add(tgt, idx, src, axis=0):
    return tgt.clone().index_add_(axis, idx, src)

def _segment_sum(src, seg, n):
    out = torch.zeros((n,) + src.shape[1:], dtype=src.dtype)
    return out.index_add_(0, seg, src)

def _pad(x, pw):
    flat = tuple((v for pair in reversed(pw) for v in pair))
    return torch.nn.functional.pad(x, flat)
kernel_name = 'adaLN_cond__memory_aug_recurrent_mem_tr__mixer_token_mlp__prompt'

def setup_inputs():

    _w = lambda a: (lambda t: t.to(torch.bfloat16) if t.is_floating_point() else t)(torch.as_tensor(a))

    def _A_setup():
        B, S, Dm = (1, 2048, 512)
        H, Dh = (32, 128)
        M, seg = (8, 32)
        seg = min(seg, S)
        S = S // seg * seg
        return {'x': _w((lambda *s: torch.randn(*s, dtype=torch.bfloat16) * 0.05)(B, S, Dm)), 'mem0': _w((lambda *s: torch.randn(*s, dtype=torch.bfloat16) * 0.4)(B, M, Dm)), 'Wq': _w((lambda *s: torch.randn(*s, dtype=torch.bfloat16) * 1.6)(Dm, H, Dh) * Dm ** (-0.5)), 'Wk': _w((lambda *s: torch.randn(*s, dtype=torch.bfloat16) * 1.6)(Dm, H, Dh) * Dm ** (-0.5)), 'Wv': _w((lambda *s: torch.randn(*s, dtype=torch.bfloat16) * 0.4)(Dm, H, Dh) * Dm ** (-0.5)), 'Wo': _w((lambda *s: torch.randn(*s, dtype=torch.bfloat16) * 0.4)(H, Dh, Dm) * (H * Dh) ** (-0.5)), 'seg_len': seg}
    _A_in = _A_setup()

    def _B_setup():
        B, S, D, F = (1, 2048, 512, 1792)
        return {'x': _w((lambda *s: torch.randn(*s, dtype=torch.bfloat16) * 0.05)(B, S, D)), 'w1': _w((lambda *s: torch.randn(*s, dtype=torch.bfloat16) * 0.8)(S, F) * S ** (-0.5)), 'b1': _w((lambda *s: torch.randn(*s, dtype=torch.bfloat16) * 12.8)(F) * 0.02), 'w2': _w((lambda *s: torch.randn(*s, dtype=torch.bfloat16) * 0.2)(F, S) * F ** (-0.5)), 'b2': _w((lambda *s: torch.randn(*s, dtype=torch.bfloat16) * 1.6)(S) * 0.02)}
    _B_in = _B_setup()
    return {'A_x': _A_in['x'], 'A_mem0': _A_in['mem0'], 'A_Wq': _A_in['Wq'], 'A_Wk': _A_in['Wk'], 'A_Wv': _A_in['Wv'], 'A_Wo': _A_in['Wo'], 'A_seg_len': _A_in['seg_len'], 'B_x': _B_in['x'], 'B_w1': _B_in['w1'], 'B_b1': _B_in['b1'], 'B_w2': _B_in['w2'], 'B_b2': _B_in['b2'], 'c': _w((lambda *s: torch.randn(*s, dtype=torch.bfloat16) * 0.1)(1, 512) * 8.0), 'w_mod': _w((lambda *s: torch.randn(*s, dtype=torch.bfloat16) * 0.1)(512, 6 * 512) * 512 ** (-0.5) * 8.0)}

def reference(A_x, A_mem0, A_Wq, A_Wk, A_Wv, A_Wo, A_seg_len, B_x, B_w1, B_b1, B_w2, B_b2, c, w_mod):

    def _A_ref(x, mem0, Wq, Wk, Wv, Wo, seg_len):
        B, S, Dm = x.shape
        H, Dh = (Wq.shape[1], Wq.shape[2])
        M = mem0.shape[1]
        scale = Dh ** (-0.5)
        n_seg = S // seg_len
        L = M + seg_len
        idx = torch.arange(L)
        mask = (idx[:, None] < M) | (idx[:, None] >= idx[None, :])
        mem = mem0
        outs = []
        for s in range(n_seg):
            xs = x[:, s * seg_len:(s + 1) * seg_len, :].clone()
            h = torch.concatenate([mem, xs], axis=1)
            q = torch.einsum('btd,dhe->bhte', h, Wq)
            k = torch.einsum('btd,dhe->bhte', h, Wk)
            v = torch.einsum('btd,dhe->bhte', h, Wv)
            scores = torch.einsum('bhqe,bhke->bhqk', q, k) * scale
            scores = torch.where(mask[None, None], scores, -1000000000.0)
            p = _softmax(scores, axis=-1)
            o = torch.einsum('bhqk,bhke->bhqe', p, v)
            o = torch.einsum('bhte,hed->btd', o, Wo)
            mem = o[:, :M, :].clone()
            outs.append(o[:, M:, :])
        return torch.concatenate(outs, axis=1)

    def _B_ref(x, w1, b1, w2, b2):
        B, S, D = x.shape
        h = _layernorm(x, axis=-1)
        u = torch.einsum('bsd,sf->bdf', h, w1) + b1
        u = _gelu(u)
        y = torch.einsum('bdf,fs->bds', u, w2) + b2
        return (lambda x, *d: x.permute(*d))(y, 0, 2, 1)
    x = A_x
    mod = (_silu(c) @ w_mod).reshape(c.shape[0], 6, 1, -1)
    sa, ca, ga, sb, cb, gb = (mod[:, 0].clone(), mod[:, 1].clone(), mod[:, 2].clone(), mod[:, 3].clone(), mod[:, 4].clone(), mod[:, 5].clone())
    h = _layernorm(x, axis=-1) * (1 + ca) + sa
    x = x + ga * _A_ref(h, A_mem0, A_Wq, A_Wk, A_Wv, A_Wo, A_seg_len)
    h = _layernorm(x, axis=-1) * (1 + cb) + sb
    x = x + gb * _B_ref(h, B_w1, B_b1, B_w2, B_b2)
    return x
```

```cpp
#include <hip/hip_runtime.h>
#include <hip/hip_bf16.h>
#include <stdint.h>

typedef unsigned short u16;
typedef unsigned int u32;
typedef __bf16 bf16x8 __attribute__((ext_vector_type(8)));
typedef __bf16 bf16x4 __attribute__((ext_vector_type(4)));
typedef __bf16 bf16x2 __attribute__((ext_vector_type(2)));
typedef float f32x4 __attribute__((ext_vector_type(4)));
typedef float f32x2 __attribute__((ext_vector_type(2)));
typedef u32 u32x2 __attribute__((ext_vector_type(2)));
typedef u32 u32x4 __attribute__((ext_vector_type(4)));
typedef short s16x4 __attribute__((ext_vector_type(4)));
typedef __amdgpu_buffer_rsrc_t rsrc_t;
#define LAS __attribute__((address_space(3)))
#define GAS __attribute__((address_space(1)))
#define DI __device__ __forceinline__
#define MFMA(a, b, c) __builtin_amdgcn_mfma_f32_16x16x32_bf16((a), (b), (c), 0, 0, 0)
#define RLX_AGENT __ATOMIC_RELAXED, __HIP_MEMORY_SCOPE_AGENT

constexpr int S_ = 2048, D_ = 512, HE_ = 4096, F_ = 1792;

constexpr u32 MB = 1u << 20;
constexpr u32 WS_MAIL = 0;
constexpr u32 WS_CTL = 2 * MB;
constexpr u32 C_CALM = WS_CTL + 12800;
constexpr u32 C_CAL = WS_CTL + 13312;
constexpr u32 C_P1 = WS_CTL + 16384;
constexpr u32 C_CP = WS_CTL + 20480;
constexpr u32 C_FX = WS_CTL + 28672;
constexpr u32 C_FY = WS_CTL + 40960;
constexpr u32 C_FU = WS_CTL + 65536;
constexpr u32 C_MOD2 = WS_CTL + 57344;
constexpr u32 CTL_BYTES = 65536 + 8192;
constexpr u32 WS_EP = WS_CTL + CTL_BYTES;
constexpr u32 WS_DN = WS_EP + 128;
constexpr u32 C_HF = WS_EP + 4096;
constexpr u32 C_MODP = C_HF + 8192;
constexpr u32 WS_DBG = 2 * MB + 131072;
constexpr u32 WS_H = 3 * MB;
constexpr u32 WS_X1 = 5 * MB;
constexpr u32 WS_H2 = 7 * MB;
constexpr u32 WS_U = 9 * MB;
constexpr u32 WS_KM = 12 * MB;
constexpr u32 WS_Q = 32 * MB;
constexpr u32 WS_KV = 48 * MB;
constexpr u32 WS_PX = 80 * MB;

DI float bf2f(u16 b) { return __uint_as_float((u32)b << 16); }
DI float blo(u32 x) { return __uint_as_float(x << 16); }
DI float bhi(u32 x) { return __uint_as_float(x & 0xffff0000u); }
DI u32 pk2(float lo, float hi) { f32x2 v = {lo, hi}; bf16x2 r = __builtin_convertvector(v, bf16x2); return __builtin_bit_cast(u32, r); }
DI float rbf(float f) { return blo(pk2(f, 0.f)); }
DI u32x4 ld4(rsrc_t r, u32 voff, u32 soff) { return __builtin_amdgcn_raw_buffer_load_b128(r, voff, soff, 16); }
DI u32x2 ld2(rsrc_t r, u32 voff, u32 soff) { return __builtin_amdgcn_raw_buffer_load_b64(r, voff, soff, 16); }
DI u32 ld1(rsrc_t r, u32 voff, u32 soff) { return __builtin_amdgcn_raw_buffer_load_b32(r, voff, soff, 16); }
DI void st4(rsrc_t r, u32x4 v, u32 voff, u32 soff) { __builtin_amdgcn_raw_buffer_store_b128(v, r, voff, soff, 16);
    asm volatile("s_nop 1" :: "v"(v) : "memory"); }
DI void st1(rsrc_t r, u32 v, u32 voff, u32 soff) { __builtin_amdgcn_raw_buffer_store_b32(v, r, voff, soff, 16); }
DI u32x4 guard(u32x4 v) { return (u32x4){min(v.x, 0xFFFFFFFEu), min(v.y, 0xFFFFFFFEu), min(v.z, 0xFFFFFFFEu), min(v.w, 0xFFFFFFFEu)}; }
DI u32 mx4(u32 m, u32x4 v) { return max(max(m, max(v.x, v.y)), max(v.z, v.w)); }
#define RKV 64
#define RPX 64
#define KVS(s) ((s) & (RKV - 1))
#define PXS(s) ((s) >= FAST0 ? RPX + (s) - FAST0 : ((s) & (RPX - 1)))
constexpr int FAST0 = 60;
#define ALLFF ((u32x4){0xFFFFFFFFu, 0xFFFFFFFFu, 0xFFFFFFFFu, 0xFFFFFFFFu})
DI bf16x8 asf(u32x4 v) { return __builtin_bit_cast(bf16x8, v); }
#define DRAIN() asm volatile("s_waitcnt vmcnt(0)" ::: "memory")
#define DRAIN_DMA() do { asm volatile("s_waitcnt vmcnt(0)" ::: "memory"); __builtin_amdgcn_s_waitcnt(0x0F70); } while (0)
#define CFENCE() asm volatile("" ::: "memory")
#define SCHED() __builtin_amdgcn_sched_barrier(0)
#define PIN_A(x) asm volatile("" : "+a"(x))
#define LBAR() do { asm volatile("s_waitcnt lgkmcnt(0)" ::: "memory"); __builtin_amdgcn_s_barrier(); asm volatile("" ::: "memory"); } while (0)
constexpr u32 SPIN_CAP = 1u << 17;
DI void wait_ge(rsrc_t r, u32 voff, int want, int slp) {
#pragma unroll 1
    for (u32 sp = 0; sp < SPIN_CAP; ++sp) {
        CFENCE();
        const int x = (int)ld1(r, voff, 0);
        if (__all(x >= want)) break;
        if (slp == 2) __builtin_amdgcn_s_sleep(2); else if (slp == 4) __builtin_amdgcn_s_sleep(4); else __builtin_amdgcn_s_sleep(16);
    }
    CFENCE();
}
DI int wait_min2(rsrc_t r, u32 voff, int want) {
    int m = want;
#pragma unroll 1
    for (u32 sp = 0; sp < SPIN_CAP; ++sp) {
        CFENCE();
        const int x = (int)ld1(r, voff, 0);
        m = min(__builtin_amdgcn_readlane(x, 0), __builtin_amdgcn_readlane(x, 1));
        if (m >= want) break;
        __builtin_amdgcn_s_sleep(4);
    }
    CFENCE();
    return m;
}
DI int wait_min6(rsrc_t r, u32 off, int want) {
    int m = want;
#pragma unroll 1
    for (u32 sp = 0; sp < SPIN_CAP; ++sp) {
        CFENCE();
        const u32x4 x = ld4(r, off, 0); const u32x2 y = ld2(r, off + 16, 0);
        m = __builtin_amdgcn_readfirstlane(min(min(min((int)x.x, (int)x.y), min((int)x.z, (int)x.w)), min((int)y.x, (int)y.y)));
        if (m >= want) break;
        __builtin_amdgcn_s_sleep(8);
    }
    CFENCE();
    return m;
}
constexpr int XPH = 2;
#define ZSLEEP 20
#define XHINT 3
#define YHINT 1
#define PACE 6
#define ZHINT 5
#define G2_T0 800
DI void wait_tag(rsrc_t r, u32 voff, u32 tag) {
#pragma unroll 1
    for (u32 sp = 0; sp < SPIN_CAP; ++sp) {
        CFENCE();
        const u32x2 x = ld2(r, voff, 0);
        if (__all(x.x == tag && x.y == ~tag)) break;
        __builtin_amdgcn_s_sleep(2);
    }
    CFENCE();
}
DI void wait_init(rsrc_t r, u32 tag, int lane) { wait_tag(r, WS_DN + (lane & 15) * 128, tag); }
DI void st2(rsrc_t r, u32 a, u32 b, u32 voff) { __builtin_amdgcn_raw_buffer_store_b64((u32x2){a, b}, r, voff, 0, 16); }
DI bf16x8 trfrag(const unsigned char* p0, const unsigned char* p1) {
    const s16x4 a = __builtin_amdgcn_ds_read_tr16_b64_v4i16((LAS s16x4*)p0), b = __builtin_amdgcn_ds_read_tr16_b64_v4i16((LAS s16x4*)p1);
    return __builtin_bit_cast(bf16x8, __builtin_shufflevector(a, b, 0, 1, 2, 3, 4, 5, 6, 7));
}
DI u32 swz(u32 row) { return ((row & 3u) << 2) | ((row >> 2) & 3u); }
DI u32 imgoff(u32 row, u32 ch) { return row * 256u + 16u * (ch ^ swz(row)); }

struct Params {
    const u16 *x, *mem0, *Wq, *Wk, *Wv, *Wo, *w1, *b1, *w2, *b2, *c, *wm;
    u16* out; unsigned char* ws;
};

#define RS_(i) do {} while (0)
DI uint64_t uni(const void* q) { const uint64_t a = (uint64_t)q; return (uint64_t)(u32)__builtin_amdgcn_readfirstlane((u32)a) | ((uint64_t)(u32)__builtin_amdgcn_readfirstlane((u32)(a >> 32)) << 32); }
#define CH_L_MST 0
#define CH_L_F 8192
#define CH_L_KV 15872
#define CH_L_CT 24064
#define CH_L_WL 32256
#define CH_FS 320
#define CH_NCONST 12
#define CH_RW 1
#define CH_CAL_OFF 2110464
#define CH_EP_OFF 2170880
#define CHAIN_ASM_TEXT \
  "s_nop 4\n\t" \
  "s_mov_b32 s71, m0\n\t" \
  "s_mov_b64 s[44:45], %0\n\t" \
  "s_mov_b32 s46, 0x10000000\n\t" \
  "s_mov_b32 s47, 0x20000\n\t" \
  "s_mov_b64 s[76:77], %2\n\t" \
  "s_mov_b32 s78, 0x400000\n\t" \
  "s_mov_b32 s79, 0x20000\n\t" \
  "s_mov_b64 s[80:81], %10\n\t" \
  "s_mov_b32 s82, 0x400000\n\t" \
  "s_mov_b32 s83, 0x20000\n\t" \
  "s_mov_b64 s[84:85], %11\n\t" \
  "s_mov_b32 s86, 0x400000\n\t" \
  "s_mov_b32 s87, 0x20000\n\t" \
  "s_mov_b64 s[88:89], %12\n\t" \
  "s_mov_b32 s90, 0x400000\n\t" \
  "s_mov_b32 s91, 0x20000\n\t" \
  "v_mbcnt_lo_u32_b32 v14, -1, 0\n\t" \
  "v_mbcnt_hi_u32_b32 v14, -1, v14\n\t" \
  "v_lshlrev_b32 v14, 2, v14\n\t" \
  "v_add_u32 v14, %1, v14\n\t" \
  "ds_read_b32 v0, v14 offset:0\n\t" \
  "ds_read_b32 v1, v14 offset:256\n\t" \
  "ds_read_b32 v2, v14 offset:512\n\t" \
  "ds_read_b32 v3, v14 offset:768\n\t" \
  "ds_read_b32 v4, v14 offset:1024\n\t" \
  "ds_read_b32 v5, v14 offset:1280\n\t" \
  "ds_read_b32 v6, v14 offset:1536\n\t" \
  "ds_read_b32 v7, v14 offset:1792\n\t" \
  "ds_read_b32 v8, v14 offset:2048\n\t" \
  "ds_read_b32 v9, v14 offset:2304\n\t" \
  "ds_read_b32 v10, v14 offset:2560\n\t" \
  "ds_read_b32 v11, v14 offset:2816\n\t" \
  "ds_read_b32 v95, v14 offset:3072\n\t" \
  "ds_read_b32 v99, v14 offset:3328\n\t" \
  "ds_read_b32 v103, v14 offset:3584\n\t" \
  "ds_read_b32 v107, v14 offset:3840\n\t" \
  "ds_read_b32 v16, v14 offset:4096\n\t" \
  "ds_read_b32 v17, v14 offset:4352\n\t" \
  "ds_read_b32 v18, v14 offset:4608\n\t" \
  "ds_read_b32 v19, v14 offset:4864\n\t" \
  "ds_read_b32 v20, v14 offset:5120\n\t" \
  "s_waitcnt lgkmcnt(0)\n\t" \
  "s_mov_b32 s72, 0xCCCCCCCC\n\t" \
  "s_mov_b32 s73, 0xCCCCCCCC\n\t" \
  "s_sub_u32 s74, 0, %6\n\t" \
  "s_mov_b32 s75, s74\n\t" \
  "s_mov_b32 s68, 0x3e0293ee\n\t" \
  "s_mov_b32 s60, 0\n\t" \
  "s_mov_b32 s61, 0\n\t" \
  "s_mov_b32 s63, 0\n\t" \
  "s_add_u32 s65, %8, 262144\n\t" \
  "s_lshl_b32 s94, %5, 10\n\t" \
  "s_add_u32 s94, s94, %4\n\t" \
  "s_add_u32 s95, s94, 524288\n\t" \
  "s_barrier\n\t" \
  "v_xor_b32 v21, 128, v16\n\t" \
  "v_xor_b32 v22, 128, v17\n\t" \
  "s_lshl_b32 s50, %5, 10\n\t" \
  "s_add_u32 s50, s50, 32256\n\t" \
  "s_mov_b32 s67, 0\n\t" \
  "s_mov_b32 s51, 0\n\t" \
  "CH_IL1_%=:\n\t" \
  "s_lshl_b32 s53, %6, 2\n\t" \
  "s_xor_b32 s53, s53, s51\n\t" \
  "s_lshl_b32 s54, s53, 13\n\t" \
  "s_add_u32 s54, s54, s50\n\t" \
  "s_lshl_b32 s67, s53, 19\n\t" \
  "s_add_u32 s67, s67, 0\n\t" \
  "s_mov_b32 m0, s54\n\t" \
  "s_add_u32 s52, s67, 131072\n\t" \
  "buffer_load_dwordx4 v16, s[80:83], s67 offen offset:0 lds\n\t" \
  "s_add_u32 m0, s54, 4096\n\t" \
  "s_add_u32 s51, s51, 1\n\t" \
  "buffer_load_dwordx4 v21, s[80:83], s52 offen offset:0 lds\n\t" \
  "s_cmp_lt_u32 s51, 8\n\t" \
  "s_cbranch_scc1 CH_IL1_%=\n\t" \
  "s_lshl_b32 s50, %5, 10\n\t" \
  "s_add_u32 s50, s50, 97792\n\t" \
  "s_mov_b32 s67, 0\n\t" \
  "s_mov_b32 s51, 0\n\t" \
  "CH_IL2_%=:\n\t" \
  "s_lshl_b32 s53, %6, 2\n\t" \
  "s_xor_b32 s53, s53, s51\n\t" \
  "s_lshl_b32 s54, s53, 13\n\t" \
  "s_add_u32 s54, s54, s50\n\t" \
  "s_lshl_b32 s67, s53, 19\n\t" \
  "s_add_u32 s67, s67, 262144\n\t" \
  "s_mov_b32 m0, s54\n\t" \
  "s_add_u32 s52, s67, 131072\n\t" \
  "buffer_load_dwordx4 v16, s[80:83], s67 offen offset:0 lds\n\t" \
  "s_add_u32 m0, s54, 4096\n\t" \
  "s_add_u32 s51, s51, 1\n\t" \
  "buffer_load_dwordx4 v21, s[80:83], s52 offen offset:0 lds\n\t" \
  "s_cmp_lt_u32 s51, 8\n\t" \
  "s_cbranch_scc1 CH_IL2_%=\n\t" \
  "s_lshl_b32 s67, %5, 1\n\t" \
  "s_add_u32 s67, s67, 0\n\t" \
  "s_and_b32 s67, s67, 7\n\t" \
  "s_lshl_b32 s67, s67, 13\n\t" \
  "v_add_u32 v108, s67, v18\n\t" \
  "v_xor_b32 v109, 32, v108\n\t" \
  "s_lshl_b32 s67, %5, 1\n\t" \
  "s_add_u32 s67, s67, 1\n\t" \
  "s_and_b32 s67, s67, 7\n\t" \
  "s_lshl_b32 s67, s67, 13\n\t" \
  "v_add_u32 v110, s67, v18\n\t" \
  "v_xor_b32 v111, 32, v110\n\t" \
  "s_lshl_b32 s67, %5, 1\n\t" \
  "s_add_u32 s67, s67, 2\n\t" \
  "s_and_b32 s67, s67, 7\n\t" \
  "s_lshl_b32 s67, s67, 13\n\t" \
  "v_add_u32 v112, s67, v18\n\t" \
  "v_xor_b32 v113, 32, v112\n\t" \
  "s_lshl_b32 s67, %5, 1\n\t" \
  "s_add_u32 s67, s67, 3\n\t" \
  "s_and_b32 s67, s67, 7\n\t" \
  "s_lshl_b32 s67, s67, 13\n\t" \
  "v_add_u32 v114, s67, v18\n\t" \
  "v_xor_b32 v115, 32, v114\n\t" \
  "s_lshl_b32 s67, %5, 1\n\t" \
  "s_add_u32 s67, s67, 4\n\t" \
  "s_and_b32 s67, s67, 7\n\t" \
  "s_lshl_b32 s67, s67, 13\n\t" \
  "v_add_u32 v116, s67, v18\n\t" \
  "v_xor_b32 v117, 32, v116\n\t" \
  "s_lshl_b32 s67, %5, 1\n\t" \
  "s_add_u32 s67, s67, 5\n\t" \
  "s_and_b32 s67, s67, 7\n\t" \
  "s_lshl_b32 s67, s67, 13\n\t" \
  "v_add_u32 v118, s67, v18\n\t" \
  "v_xor_b32 v119, 32, v118\n\t" \
  "s_lshl_b32 s67, %5, 1\n\t" \
  "s_add_u32 s67, s67, 6\n\t" \
  "s_and_b32 s67, s67, 7\n\t" \
  "s_lshl_b32 s67, s67, 13\n\t" \
  "v_add_u32 v120, s67, v18\n\t" \
  "v_xor_b32 v121, 32, v120\n\t" \
  "s_lshl_b32 s67, %5, 1\n\t" \
  "s_add_u32 s67, s67, 7\n\t" \
  "s_and_b32 s67, s67, 7\n\t" \
  "s_lshl_b32 s67, s67, 13\n\t" \
  "v_add_u32 v122, s67, v18\n\t" \
  "v_xor_b32 v123, 32, v122\n\t" \
  "s_waitcnt vmcnt(16)\n\t" \
  "s_barrier\n\t" \
  "ds_read_b64_tr_b16 a[0:1], v108 offset:0\n\t" \
  "ds_read_b64_tr_b16 a[4:5], v108 offset:2048\n\t" \
  "ds_read_b64_tr_b16 a[8:9], v109 offset:0\n\t" \
  "ds_read_b64_tr_b16 a[12:13], v109 offset:2048\n\t" \
  "ds_read_b64_tr_b16 a[16:17], v110 offset:0\n\t" \
  "ds_read_b64_tr_b16 a[20:21], v110 offset:2048\n\t" \
  "ds_read_b64_tr_b16 a[24:25], v111 offset:0\n\t" \
  "ds_read_b64_tr_b16 a[28:29], v111 offset:2048\n\t" \
  "ds_read_b64_tr_b16 a[32:33], v112 offset:0\n\t" \
  "ds_read_b64_tr_b16 a[36:37], v112 offset:2048\n\t" \
  "ds_read_b64_tr_b16 a[40:41], v113 offset:0\n\t" \
  "ds_read_b64_tr_b16 a[44:45], v113 offset:2048\n\t" \
  "ds_read_b64_tr_b16 a[48:49], v114 offset:0\n\t" \
  "ds_read_b64_tr_b16 a[52:53], v114 offset:2048\n\t" \
  "ds_read_b64_tr_b16 a[56:57], v115 offset:0\n\t" \
  "ds_read_b64_tr_b16 a[60:61], v115 offset:2048\n\t" \
  "ds_read_b64_tr_b16 a[64:65], v116 offset:0\n\t" \
  "ds_read_b64_tr_b16 a[68:69], v116 offset:2048\n\t" \
  "ds_read_b64_tr_b16 a[72:73], v117 offset:0\n\t" \
  "ds_read_b64_tr_b16 a[76:77], v117 offset:2048\n\t" \
  "ds_read_b64_tr_b16 a[80:81], v118 offset:0\n\t" \
  "ds_read_b64_tr_b16 a[84:85], v118 offset:2048\n\t" \
  "ds_read_b64_tr_b16 a[88:89], v119 offset:0\n\t" \
  "ds_read_b64_tr_b16 a[92:93], v119 offset:2048\n\t" \
  "ds_read_b64_tr_b16 a[96:97], v120 offset:0\n\t" \
  "ds_read_b64_tr_b16 a[100:101], v120 offset:2048\n\t" \
  "ds_read_b64_tr_b16 a[104:105], v121 offset:0\n\t" \
  "ds_read_b64_tr_b16 a[108:109], v121 offset:2048\n\t" \
  "ds_read_b64_tr_b16 a[112:113], v122 offset:0\n\t" \
  "ds_read_b64_tr_b16 a[116:117], v122 offset:2048\n\t" \
  "ds_read_b64_tr_b16 a[120:121], v123 offset:0\n\t" \
  "ds_read_b64_tr_b16 a[124:125], v123 offset:2048\n\t" \
  "v_add_u32 v108, 0x10000, v108\n\t" \
  "v_add_u32 v109, 0x10000, v109\n\t" \
  "v_add_u32 v110, 0x10000, v110\n\t" \
  "v_add_u32 v111, 0x10000, v111\n\t" \
  "v_add_u32 v112, 0x10000, v112\n\t" \
  "v_add_u32 v113, 0x10000, v113\n\t" \
  "v_add_u32 v114, 0x10000, v114\n\t" \
  "v_add_u32 v115, 0x10000, v115\n\t" \
  "v_add_u32 v116, 0x10000, v116\n\t" \
  "v_add_u32 v117, 0x10000, v117\n\t" \
  "v_add_u32 v118, 0x10000, v118\n\t" \
  "v_add_u32 v119, 0x10000, v119\n\t" \
  "v_add_u32 v120, 0x10000, v120\n\t" \
  "v_add_u32 v121, 0x10000, v121\n\t" \
  "v_add_u32 v122, 0x10000, v122\n\t" \
  "v_add_u32 v123, 0x10000, v123\n\t" \
  "s_waitcnt lgkmcnt(0)\n\t" \
  "s_barrier\n\t" \
  "s_lshl_b32 s50, %5, 10\n\t" \
  "s_add_u32 s50, s50, 32256\n\t" \
  "s_mov_b32 s67, 0\n\t" \
  "s_mov_b32 s51, 0\n\t" \
  "CH_IL3_%=:\n\t" \
  "s_lshl_b32 s53, %6, 2\n\t" \
  "s_xor_b32 s53, s53, s51\n\t" \
  "s_lshl_b32 s54, s53, 13\n\t" \
  "s_add_u32 s54, s54, s50\n\t" \
  "s_lshl_b32 s67, s53, 19\n\t" \
  "s_add_u32 s67, s67, 0\n\t" \
  "s_mov_b32 m0, s54\n\t" \
  "s_add_u32 s52, s67, 131072\n\t" \
  "buffer_load_dwordx4 v16, s[84:87], s67 offen offset:0 lds\n\t" \
  "s_add_u32 m0, s54, 4096\n\t" \
  "s_add_u32 s51, s51, 1\n\t" \
  "buffer_load_dwordx4 v21, s[84:87], s52 offen offset:0 lds\n\t" \
  "s_cmp_lt_u32 s51, 8\n\t" \
  "s_cbranch_scc1 CH_IL3_%=\n\t" \
  "s_waitcnt vmcnt(16)\n\t" \
  "s_barrier\n\t" \
  "ds_read_b64_tr_b16 a[2:3], v108 offset:0\n\t" \
  "ds_read_b64_tr_b16 a[6:7], v108 offset:2048\n\t" \
  "ds_read_b64_tr_b16 a[10:11], v109 offset:0\n\t" \
  "ds_read_b64_tr_b16 a[14:15], v109 offset:2048\n\t" \
  "ds_read_b64_tr_b16 a[18:19], v110 offset:0\n\t" \
  "ds_read_b64_tr_b16 a[22:23], v110 offset:2048\n\t" \
  "ds_read_b64_tr_b16 a[26:27], v111 offset:0\n\t" \
  "ds_read_b64_tr_b16 a[30:31], v111 offset:2048\n\t" \
  "ds_read_b64_tr_b16 a[34:35], v112 offset:0\n\t" \
  "ds_read_b64_tr_b16 a[38:39], v112 offset:2048\n\t" \
  "ds_read_b64_tr_b16 a[42:43], v113 offset:0\n\t" \
  "ds_read_b64_tr_b16 a[46:47], v113 offset:2048\n\t" \
  "ds_read_b64_tr_b16 a[50:51], v114 offset:0\n\t" \
  "ds_read_b64_tr_b16 a[54:55], v114 offset:2048\n\t" \
  "ds_read_b64_tr_b16 a[58:59], v115 offset:0\n\t" \
  "ds_read_b64_tr_b16 a[62:63], v115 offset:2048\n\t" \
  "ds_read_b64_tr_b16 a[66:67], v116 offset:0\n\t" \
  "ds_read_b64_tr_b16 a[70:71], v116 offset:2048\n\t" \
  "ds_read_b64_tr_b16 a[74:75], v117 offset:0\n\t" \
  "ds_read_b64_tr_b16 a[78:79], v117 offset:2048\n\t" \
  "ds_read_b64_tr_b16 a[82:83], v118 offset:0\n\t" \
  "ds_read_b64_tr_b16 a[86:87], v118 offset:2048\n\t" \
  "ds_read_b64_tr_b16 a[90:91], v119 offset:0\n\t" \
  "ds_read_b64_tr_b16 a[94:95], v119 offset:2048\n\t" \
  "ds_read_b64_tr_b16 a[98:99], v120 offset:0\n\t" \
  "ds_read_b64_tr_b16 a[102:103], v120 offset:2048\n\t" \
  "ds_read_b64_tr_b16 a[106:107], v121 offset:0\n\t" \
  "ds_read_b64_tr_b16 a[110:111], v121 offset:2048\n\t" \
  "ds_read_b64_tr_b16 a[114:115], v122 offset:0\n\t" \
  "ds_read_b64_tr_b16 a[118:119], v122 offset:2048\n\t" \
  "ds_read_b64_tr_b16 a[122:123], v123 offset:0\n\t" \
  "ds_read_b64_tr_b16 a[126:127], v123 offset:2048\n\t" \
  "v_subrev_u32 v108, 0x10000, v108\n\t" \
  "v_subrev_u32 v109, 0x10000, v109\n\t" \
  "v_subrev_u32 v110, 0x10000, v110\n\t" \
  "v_subrev_u32 v111, 0x10000, v111\n\t" \
  "v_subrev_u32 v112, 0x10000, v112\n\t" \
  "v_subrev_u32 v113, 0x10000, v113\n\t" \
  "v_subrev_u32 v114, 0x10000, v114\n\t" \
  "v_subrev_u32 v115, 0x10000, v115\n\t" \
  "v_subrev_u32 v116, 0x10000, v116\n\t" \
  "v_subrev_u32 v117, 0x10000, v117\n\t" \
  "v_subrev_u32 v118, 0x10000, v118\n\t" \
  "v_subrev_u32 v119, 0x10000, v119\n\t" \
  "v_subrev_u32 v120, 0x10000, v120\n\t" \
  "v_subrev_u32 v121, 0x10000, v121\n\t" \
  "v_subrev_u32 v122, 0x10000, v122\n\t" \
  "v_subrev_u32 v123, 0x10000, v123\n\t" \
  "s_waitcnt lgkmcnt(0)\n\t" \
  "s_barrier\n\t" \
  "s_lshl_b32 s50, %5, 10\n\t" \
  "s_add_u32 s50, s50, 97792\n\t" \
  "s_mov_b32 s67, 0\n\t" \
  "s_mov_b32 s51, 0\n\t" \
  "CH_IL4_%=:\n\t" \
  "s_lshl_b32 s53, %6, 2\n\t" \
  "s_xor_b32 s53, s53, s51\n\t" \
  "s_lshl_b32 s54, s53, 13\n\t" \
  "s_add_u32 s54, s54, s50\n\t" \
  "s_lshl_b32 s67, s53, 19\n\t" \
  "s_add_u32 s67, s67, 262144\n\t" \
  "s_mov_b32 m0, s54\n\t" \
  "s_add_u32 s52, s67, 131072\n\t" \
  "buffer_load_dwordx4 v16, s[84:87], s67 offen offset:0 lds\n\t" \
  "s_add_u32 m0, s54, 4096\n\t" \
  "s_add_u32 s51, s51, 1\n\t" \
  "buffer_load_dwordx4 v21, s[84:87], s52 offen offset:0 lds\n\t" \
  "s_cmp_lt_u32 s51, 8\n\t" \
  "s_cbranch_scc1 CH_IL4_%=\n\t" \
  "s_waitcnt vmcnt(16)\n\t" \
  "s_barrier\n\t" \
  "ds_read_b64_tr_b16 a[128:129], v108 offset:0\n\t" \
  "ds_read_b64_tr_b16 a[132:133], v108 offset:2048\n\t" \
  "ds_read_b64_tr_b16 a[136:137], v109 offset:0\n\t" \
  "ds_read_b64_tr_b16 a[140:141], v109 offset:2048\n\t" \
  "ds_read_b64_tr_b16 a[144:145], v110 offset:0\n\t" \
  "ds_read_b64_tr_b16 a[148:149], v110 offset:2048\n\t" \
  "ds_read_b64_tr_b16 a[152:153], v111 offset:0\n\t" \
  "ds_read_b64_tr_b16 a[156:157], v111 offset:2048\n\t" \
  "ds_read_b64_tr_b16 a[160:161], v112 offset:0\n\t" \
  "ds_read_b64_tr_b16 a[164:165], v112 offset:2048\n\t" \
  "ds_read_b64_tr_b16 a[168:169], v113 offset:0\n\t" \
  "ds_read_b64_tr_b16 a[172:173], v113 offset:2048\n\t" \
  "ds_read_b64_tr_b16 a[176:177], v114 offset:0\n\t" \
  "ds_read_b64_tr_b16 a[180:181], v114 offset:2048\n\t" \
  "ds_read_b64_tr_b16 a[184:185], v115 offset:0\n\t" \
  "ds_read_b64_tr_b16 a[188:189], v115 offset:2048\n\t" \
  "ds_read_b64_tr_b16 a[192:193], v116 offset:0\n\t" \
  "ds_read_b64_tr_b16 a[196:197], v116 offset:2048\n\t" \
  "ds_read_b64_tr_b16 a[200:201], v117 offset:0\n\t" \
  "ds_read_b64_tr_b16 a[204:205], v117 offset:2048\n\t" \
  "ds_read_b64_tr_b16 a[208:209], v118 offset:0\n\t" \
  "ds_read_b64_tr_b16 a[212:213], v118 offset:2048\n\t" \
  "ds_read_b64_tr_b16 a[216:217], v119 offset:0\n\t" \
  "ds_read_b64_tr_b16 a[220:221], v119 offset:2048\n\t" \
  "ds_read_b64_tr_b16 a[224:225], v120 offset:0\n\t" \
  "ds_read_b64_tr_b16 a[228:229], v120 offset:2048\n\t" \
  "ds_read_b64_tr_b16 a[232:233], v121 offset:0\n\t" \
  "ds_read_b64_tr_b16 a[236:237], v121 offset:2048\n\t" \
  "ds_read_b64_tr_b16 a[240:241], v122 offset:0\n\t" \
  "ds_read_b64_tr_b16 a[244:245], v122 offset:2048\n\t" \
  "ds_read_b64_tr_b16 a[248:249], v123 offset:0\n\t" \
  "ds_read_b64_tr_b16 a[252:253], v123 offset:2048\n\t" \
  "v_add_u32 v108, 0x10000, v108\n\t" \
  "v_add_u32 v109, 0x10000, v109\n\t" \
  "v_add_u32 v110, 0x10000, v110\n\t" \
  "v_add_u32 v111, 0x10000, v111\n\t" \
  "v_add_u32 v112, 0x10000, v112\n\t" \
  "v_add_u32 v113, 0x10000, v113\n\t" \
  "v_add_u32 v114, 0x10000, v114\n\t" \
  "v_add_u32 v115, 0x10000, v115\n\t" \
  "v_add_u32 v116, 0x10000, v116\n\t" \
  "v_add_u32 v117, 0x10000, v117\n\t" \
  "v_add_u32 v118, 0x10000, v118\n\t" \
  "v_add_u32 v119, 0x10000, v119\n\t" \
  "v_add_u32 v120, 0x10000, v120\n\t" \
  "v_add_u32 v121, 0x10000, v121\n\t" \
  "v_add_u32 v122, 0x10000, v122\n\t" \
  "v_add_u32 v123, 0x10000, v123\n\t" \
  "s_waitcnt lgkmcnt(0)\n\t" \
  "s_barrier\n\t" \
  "s_lshl_b32 s50, %5, 10\n\t" \
  "s_add_u32 s50, s50, 32256\n\t" \
  "s_mov_b32 s67, 0\n\t" \
  "s_mov_b32 s51, 0\n\t" \
  "CH_IL5_%=:\n\t" \
  "s_lshl_b32 s53, %6, 2\n\t" \
  "s_xor_b32 s53, s53, s51\n\t" \
  "s_lshl_b32 s54, s53, 13\n\t" \
  "s_add_u32 s54, s54, s50\n\t" \
  "s_lshl_b32 s67, s53, 19\n\t" \
  "s_add_u32 s67, s67, 0\n\t" \
  "s_mov_b32 m0, s54\n\t" \
  "s_add_u32 s52, s67, 131072\n\t" \
  "buffer_load_dwordx4 v16, s[76:79], s67 offen offset:0 lds\n\t" \
  "s_add_u32 m0, s54, 4096\n\t" \
  "s_add_u32 s51, s51, 1\n\t" \
  "buffer_load_dwordx4 v21, s[76:79], s52 offen offset:0 lds\n\t" \
  "s_cmp_lt_u32 s51, 8\n\t" \
  "s_cbranch_scc1 CH_IL5_%=\n\t" \
  "s_waitcnt vmcnt(16)\n\t" \
  "s_barrier\n\t" \
  "ds_read_b64_tr_b16 a[130:131], v108 offset:0\n\t" \
  "ds_read_b64_tr_b16 a[134:135], v108 offset:2048\n\t" \
  "ds_read_b64_tr_b16 a[138:139], v109 offset:0\n\t" \
  "ds_read_b64_tr_b16 a[142:143], v109 offset:2048\n\t" \
  "ds_read_b64_tr_b16 a[146:147], v110 offset:0\n\t" \
  "ds_read_b64_tr_b16 a[150:151], v110 offset:2048\n\t" \
  "ds_read_b64_tr_b16 a[154:155], v111 offset:0\n\t" \
  "ds_read_b64_tr_b16 a[158:159], v111 offset:2048\n\t" \
  "ds_read_b64_tr_b16 a[162:163], v112 offset:0\n\t" \
  "ds_read_b64_tr_b16 a[166:167], v112 offset:2048\n\t" \
  "ds_read_b64_tr_b16 a[170:171], v113 offset:0\n\t" \
  "ds_read_b64_tr_b16 a[174:175], v113 offset:2048\n\t" \
  "ds_read_b64_tr_b16 a[178:179], v114 offset:0\n\t" \
  "ds_read_b64_tr_b16 a[182:183], v114 offset:2048\n\t" \
  "ds_read_b64_tr_b16 a[186:187], v115 offset:0\n\t" \
  "ds_read_b64_tr_b16 a[190:191], v115 offset:2048\n\t" \
  "ds_read_b64_tr_b16 a[194:195], v116 offset:0\n\t" \
  "ds_read_b64_tr_b16 a[198:199], v116 offset:2048\n\t" \
  "ds_read_b64_tr_b16 a[202:203], v117 offset:0\n\t" \
  "ds_read_b64_tr_b16 a[206:207], v117 offset:2048\n\t" \
  "ds_read_b64_tr_b16 a[210:211], v118 offset:0\n\t" \
  "ds_read_b64_tr_b16 a[214:215], v118 offset:2048\n\t" \
  "ds_read_b64_tr_b16 a[218:219], v119 offset:0\n\t" \
  "ds_read_b64_tr_b16 a[222:223], v119 offset:2048\n\t" \
  "ds_read_b64_tr_b16 a[226:227], v120 offset:0\n\t" \
  "ds_read_b64_tr_b16 a[230:231], v120 offset:2048\n\t" \
  "ds_read_b64_tr_b16 a[234:235], v121 offset:0\n\t" \
  "ds_read_b64_tr_b16 a[238:239], v121 offset:2048\n\t" \
  "ds_read_b64_tr_b16 a[242:243], v122 offset:0\n\t" \
  "ds_read_b64_tr_b16 a[246:247], v122 offset:2048\n\t" \
  "ds_read_b64_tr_b16 a[250:251], v123 offset:0\n\t" \
  "ds_read_b64_tr_b16 a[254:255], v123 offset:2048\n\t" \
  "v_subrev_u32 v108, 0x10000, v108\n\t" \
  "v_subrev_u32 v109, 0x10000, v109\n\t" \
  "v_subrev_u32 v110, 0x10000, v110\n\t" \
  "v_subrev_u32 v111, 0x10000, v111\n\t" \
  "v_subrev_u32 v112, 0x10000, v112\n\t" \
  "v_subrev_u32 v113, 0x10000, v113\n\t" \
  "v_subrev_u32 v114, 0x10000, v114\n\t" \
  "v_subrev_u32 v115, 0x10000, v115\n\t" \
  "v_subrev_u32 v116, 0x10000, v116\n\t" \
  "v_subrev_u32 v117, 0x10000, v117\n\t" \
  "v_subrev_u32 v118, 0x10000, v118\n\t" \
  "v_subrev_u32 v119, 0x10000, v119\n\t" \
  "v_subrev_u32 v120, 0x10000, v120\n\t" \
  "v_subrev_u32 v121, 0x10000, v121\n\t" \
  "v_subrev_u32 v122, 0x10000, v122\n\t" \
  "v_subrev_u32 v123, 0x10000, v123\n\t" \
  "s_waitcnt lgkmcnt(0)\n\t" \
  "s_barrier\n\t" \
  "s_lshl_b32 s50, %5, 10\n\t" \
  "s_add_u32 s50, s50, 97792\n\t" \
  "s_mov_b32 s67, 0\n\t" \
  "s_mov_b32 s51, 0\n\t" \
  "CH_IL6_%=:\n\t" \
  "s_lshl_b32 s53, %6, 2\n\t" \
  "s_xor_b32 s53, s53, s51\n\t" \
  "s_lshl_b32 s54, s53, 13\n\t" \
  "s_add_u32 s54, s54, s50\n\t" \
  "s_lshl_b32 s67, s53, 19\n\t" \
  "s_add_u32 s67, s67, 262144\n\t" \
  "s_mov_b32 m0, s54\n\t" \
  "s_add_u32 s52, s67, 131072\n\t" \
  "buffer_load_dwordx4 v16, s[76:79], s67 offen offset:0 lds\n\t" \
  "s_add_u32 m0, s54, 4096\n\t" \
  "s_add_u32 s51, s51, 1\n\t" \
  "buffer_load_dwordx4 v21, s[76:79], s52 offen offset:0 lds\n\t" \
  "s_cmp_lt_u32 s51, 8\n\t" \
  "s_cbranch_scc1 CH_IL6_%=\n\t" \
  "s_waitcnt vmcnt(16)\n\t" \
  "s_barrier\n\t" \
  "ds_read_b64_tr_b16 v[128:129], v108 offset:0\n\t" \
  "ds_read_b64_tr_b16 v[132:133], v108 offset:2048\n\t" \
  "ds_read_b64_tr_b16 v[136:137], v109 offset:0\n\t" \
  "ds_read_b64_tr_b16 v[140:141], v109 offset:2048\n\t" \
  "ds_read_b64_tr_b16 v[144:145], v110 offset:0\n\t" \
  "ds_read_b64_tr_b16 v[148:149], v110 offset:2048\n\t" \
  "ds_read_b64_tr_b16 v[152:153], v111 offset:0\n\t" \
  "ds_read_b64_tr_b16 v[156:157], v111 offset:2048\n\t" \
  "ds_read_b64_tr_b16 v[160:161], v112 offset:0\n\t" \
  "ds_read_b64_tr_b16 v[164:165], v112 offset:2048\n\t" \
  "ds_read_b64_tr_b16 v[168:169], v113 offset:0\n\t" \
  "ds_read_b64_tr_b16 v[172:173], v113 offset:2048\n\t" \
  "ds_read_b64_tr_b16 v[176:177], v114 offset:0\n\t" \
  "ds_read_b64_tr_b16 v[180:181], v114 offset:2048\n\t" \
  "ds_read_b64_tr_b16 v[184:185], v115 offset:0\n\t" \
  "ds_read_b64_tr_b16 v[188:189], v115 offset:2048\n\t" \
  "ds_read_b64_tr_b16 v[192:193], v116 offset:0\n\t" \
  "ds_read_b64_tr_b16 v[196:197], v116 offset:2048\n\t" \
  "ds_read_b64_tr_b16 v[200:201], v117 offset:0\n\t" \
  "ds_read_b64_tr_b16 v[204:205], v117 offset:2048\n\t" \
  "ds_read_b64_tr_b16 v[208:209], v118 offset:0\n\t" \
  "ds_read_b64_tr_b16 v[212:213], v118 offset:2048\n\t" \
  "ds_read_b64_tr_b16 v[216:217], v119 offset:0\n\t" \
  "ds_read_b64_tr_b16 v[220:221], v119 offset:2048\n\t" \
  "ds_read_b64_tr_b16 v[224:225], v120 offset:0\n\t" \
  "ds_read_b64_tr_b16 v[228:229], v120 offset:2048\n\t" \
  "ds_read_b64_tr_b16 v[232:233], v121 offset:0\n\t" \
  "ds_read_b64_tr_b16 v[236:237], v121 offset:2048\n\t" \
  "ds_read_b64_tr_b16 v[240:241], v122 offset:0\n\t" \
  "ds_read_b64_tr_b16 v[244:245], v122 offset:2048\n\t" \
  "ds_read_b64_tr_b16 v[248:249], v123 offset:0\n\t" \
  "ds_read_b64_tr_b16 v[252:253], v123 offset:2048\n\t" \
  "v_add_u32 v108, 0x10000, v108\n\t" \
  "v_add_u32 v109, 0x10000, v109\n\t" \
  "v_add_u32 v110, 0x10000, v110\n\t" \
  "v_add_u32 v111, 0x10000, v111\n\t" \
  "v_add_u32 v112, 0x10000, v112\n\t" \
  "v_add_u32 v113, 0x10000, v113\n\t" \
  "v_add_u32 v114, 0x10000, v114\n\t" \
  "v_add_u32 v115, 0x10000, v115\n\t" \
  "v_add_u32 v116, 0x10000, v116\n\t" \
  "v_add_u32 v117, 0x10000, v117\n\t" \
  "v_add_u32 v118, 0x10000, v118\n\t" \
  "v_add_u32 v119, 0x10000, v119\n\t" \
  "v_add_u32 v120, 0x10000, v120\n\t" \
  "v_add_u32 v121, 0x10000, v121\n\t" \
  "v_add_u32 v122, 0x10000, v122\n\t" \
  "v_add_u32 v123, 0x10000, v123\n\t" \
  "s_waitcnt lgkmcnt(0)\n\t" \
  "s_barrier\n\t" \
  "s_lshl_b32 s50, %5, 10\n\t" \
  "s_add_u32 s50, s50, 32256\n\t" \
  "s_mov_b32 s67, 0\n\t" \
  "s_mov_b32 s51, 0\n\t" \
  "CH_IL7_%=:\n\t" \
  "s_add_u32 m0, s50, 0\n\t" \
  "s_add_u32 s52, s67, 0\n\t" \
  "buffer_load_dwordx4 v17, s[88:91], s52 offen offset:0 lds\n\t" \
  "s_add_u32 m0, s50, 4096\n\t" \
  "s_add_u32 s52, s67, 8192\n\t" \
  "buffer_load_dwordx4 v17, s[88:91], s52 offen offset:0 lds\n\t" \
  "s_add_u32 m0, s50, 8192\n\t" \
  "s_add_u32 s52, s67, 16384\n\t" \
  "buffer_load_dwordx4 v22, s[88:91], s52 offen offset:0 lds\n\t" \
  "s_add_u32 m0, s50, 12288\n\t" \
  "s_add_u32 s52, s67, 24576\n\t" \
  "buffer_load_dwordx4 v22, s[88:91], s52 offen offset:0 lds\n\t" \
  "s_add_u32 s67, s67, 32768\n\t" \
  "s_add_u32 s50, s50, 16384\n\t" \
  "s_add_u32 s51, s51, 1\n\t" \
  "s_cmp_lt_u32 s51, 4\n\t" \
  "s_cbranch_scc1 CH_IL7_%=\n\t" \
  "s_waitcnt vmcnt(16)\n\t" \
  "s_barrier\n\t" \
  "ds_read_b64_tr_b16 v[130:131], v108 offset:0\n\t" \
  "ds_read_b64_tr_b16 v[134:135], v108 offset:2048\n\t" \
  "ds_read_b64_tr_b16 v[138:139], v109 offset:0\n\t" \
  "ds_read_b64_tr_b16 v[142:143], v109 offset:2048\n\t" \
  "ds_read_b64_tr_b16 v[146:147], v110 offset:0\n\t" \
  "ds_read_b64_tr_b16 v[150:151], v110 offset:2048\n\t" \
  "ds_read_b64_tr_b16 v[154:155], v111 offset:0\n\t" \
  "ds_read_b64_tr_b16 v[158:159], v111 offset:2048\n\t" \
  "ds_read_b64_tr_b16 v[162:163], v112 offset:0\n\t" \
  "ds_read_b64_tr_b16 v[166:167], v112 offset:2048\n\t" \
  "ds_read_b64_tr_b16 v[170:171], v113 offset:0\n\t" \
  "ds_read_b64_tr_b16 v[174:175], v113 offset:2048\n\t" \
  "ds_read_b64_tr_b16 v[178:179], v114 offset:0\n\t" \
  "ds_read_b64_tr_b16 v[182:183], v114 offset:2048\n\t" \
  "ds_read_b64_tr_b16 v[186:187], v115 offset:0\n\t" \
  "ds_read_b64_tr_b16 v[190:191], v115 offset:2048\n\t" \
  "ds_read_b64_tr_b16 v[194:195], v116 offset:0\n\t" \
  "ds_read_b64_tr_b16 v[198:199], v116 offset:2048\n\t" \
  "ds_read_b64_tr_b16 v[202:203], v117 offset:0\n\t" \
  "ds_read_b64_tr_b16 v[206:207], v117 offset:2048\n\t" \
  "ds_read_b64_tr_b16 v[210:211], v118 offset:0\n\t" \
  "ds_read_b64_tr_b16 v[214:215], v118 offset:2048\n\t" \
  "ds_read_b64_tr_b16 v[218:219], v119 offset:0\n\t" \
  "ds_read_b64_tr_b16 v[222:223], v119 offset:2048\n\t" \
  "ds_read_b64_tr_b16 v[226:227], v120 offset:0\n\t" \
  "ds_read_b64_tr_b16 v[230:231], v120 offset:2048\n\t" \
  "ds_read_b64_tr_b16 v[234:235], v121 offset:0\n\t" \
  "ds_read_b64_tr_b16 v[238:239], v121 offset:2048\n\t" \
  "ds_read_b64_tr_b16 v[242:243], v122 offset:0\n\t" \
  "ds_read_b64_tr_b16 v[246:247], v122 offset:2048\n\t" \
  "ds_read_b64_tr_b16 v[250:251], v123 offset:0\n\t" \
  "ds_read_b64_tr_b16 v[254:255], v123 offset:2048\n\t" \
  "v_subrev_u32 v108, 0x10000, v108\n\t" \
  "v_subrev_u32 v109, 0x10000, v109\n\t" \
  "v_subrev_u32 v110, 0x10000, v110\n\t" \
  "v_subrev_u32 v111, 0x10000, v111\n\t" \
  "v_subrev_u32 v112, 0x10000, v112\n\t" \
  "v_subrev_u32 v113, 0x10000, v113\n\t" \
  "v_subrev_u32 v114, 0x10000, v114\n\t" \
  "v_subrev_u32 v115, 0x10000, v115\n\t" \
  "v_subrev_u32 v116, 0x10000, v116\n\t" \
  "v_subrev_u32 v117, 0x10000, v117\n\t" \
  "v_subrev_u32 v118, 0x10000, v118\n\t" \
  "v_subrev_u32 v119, 0x10000, v119\n\t" \
  "v_subrev_u32 v120, 0x10000, v120\n\t" \
  "v_subrev_u32 v121, 0x10000, v121\n\t" \
  "v_subrev_u32 v122, 0x10000, v122\n\t" \
  "v_subrev_u32 v123, 0x10000, v123\n\t" \
  "s_waitcnt lgkmcnt(0)\n\t" \
  "s_barrier\n\t" \
  "s_waitcnt vmcnt(0)\n\t" \
  "s_barrier\n\t" \
  "ds_read_b64_tr_b16 v[24:25], v19 offset:0\n\t" \
  "ds_read_b64_tr_b16 v[26:27], v19 offset:512\n\t" \
  "ds_read_b64_tr_b16 v[28:29], v19 offset:16384\n\t" \
  "ds_read_b64_tr_b16 v[30:31], v19 offset:16896\n\t" \
  "ds_read_b64_tr_b16 v[32:33], v19 offset:32768\n\t" \
  "ds_read_b64_tr_b16 v[34:35], v19 offset:33280\n\t" \
  "ds_read_b64_tr_b16 v[36:37], v19 offset:49152\n\t" \
  "ds_read_b64_tr_b16 v[38:39], v19 offset:49664\n\t" \
  "v_xor_b32 v14, 32, v19\n\t" \
  "ds_read_b64_tr_b16 v[40:41], v14 offset:0\n\t" \
  "ds_read_b64_tr_b16 v[42:43], v14 offset:512\n\t" \
  "ds_read_b64_tr_b16 v[44:45], v14 offset:16384\n\t" \
  "ds_read_b64_tr_b16 v[46:47], v14 offset:16896\n\t" \
  "ds_read_b64_tr_b16 v[48:49], v14 offset:32768\n\t" \
  "ds_read_b64_tr_b16 v[50:51], v14 offset:33280\n\t" \
  "ds_read_b64_tr_b16 v[52:53], v14 offset:49152\n\t" \
  "ds_read_b64_tr_b16 v[54:55], v14 offset:49664\n\t" \
  "v_xor_b32 v14, 64, v19\n\t" \
  "ds_read_b64_tr_b16 v[56:57], v14 offset:0\n\t" \
  "ds_read_b64_tr_b16 v[58:59], v14 offset:512\n\t" \
  "ds_read_b64_tr_b16 v[60:61], v14 offset:16384\n\t" \
  "ds_read_b64_tr_b16 v[62:63], v14 offset:16896\n\t" \
  "ds_read_b64_tr_b16 v[64:65], v14 offset:32768\n\t" \
  "ds_read_b64_tr_b16 v[66:67], v14 offset:33280\n\t" \
  "ds_read_b64_tr_b16 v[68:69], v14 offset:49152\n\t" \
  "ds_read_b64_tr_b16 v[70:71], v14 offset:49664\n\t" \
  "v_xor_b32 v14, 96, v19\n\t" \
  "ds_read_b64_tr_b16 v[72:73], v14 offset:0\n\t" \
  "ds_read_b64_tr_b16 v[74:75], v14 offset:512\n\t" \
  "ds_read_b64_tr_b16 v[76:77], v14 offset:16384\n\t" \
  "ds_read_b64_tr_b16 v[78:79], v14 offset:16896\n\t" \
  "ds_read_b64_tr_b16 v[80:81], v14 offset:32768\n\t" \
  "ds_read_b64_tr_b16 v[82:83], v14 offset:33280\n\t" \
  "ds_read_b64_tr_b16 v[84:85], v14 offset:49152\n\t" \
  "ds_read_b64_tr_b16 v[86:87], v14 offset:49664\n\t" \
  "s_waitcnt lgkmcnt(0)\n\t" \
  "s_barrier\n\t" \
  "ds_write_b128 v8, v[128:131] offset:0\n\t" \
  "ds_write_b128 v8, v[132:135] offset:1024\n\t" \
  "ds_write_b128 v8, v[136:139] offset:2048\n\t" \
  "ds_write_b128 v8, v[140:143] offset:3072\n\t" \
  "ds_write_b128 v8, v[144:147] offset:4096\n\t" \
  "ds_write_b128 v8, v[148:151] offset:5120\n\t" \
  "ds_write_b128 v8, v[152:155] offset:6144\n\t" \
  "ds_write_b128 v8, v[156:159] offset:7168\n\t" \
  "ds_write_b128 v8, v[160:163] offset:8192\n\t" \
  "ds_write_b128 v8, v[164:167] offset:9216\n\t" \
  "ds_write_b128 v8, v[168:171] offset:10240\n\t" \
  "ds_write_b128 v8, v[172:175] offset:11264\n\t" \
  "ds_write_b128 v8, v[176:179] offset:12288\n\t" \
  "ds_write_b128 v8, v[180:183] offset:13312\n\t" \
  "ds_write_b128 v8, v[184:187] offset:14336\n\t" \
  "ds_write_b128 v8, v[188:191] offset:15360\n\t" \
  "ds_write_b128 v8, v[24:27] offset:16384\n\t" \
  "ds_write_b128 v8, v[28:31] offset:17408\n\t" \
  "ds_write_b128 v8, v[32:35] offset:18432\n\t" \
  "ds_write_b128 v8, v[36:39] offset:19456\n\t" \
  "ds_write_b128 v8, v[40:43] offset:20480\n\t" \
  "ds_write_b128 v8, v[44:47] offset:21504\n\t" \
  "ds_write_b128 v8, v[48:51] offset:22528\n\t" \
  "ds_write_b128 v8, v[52:55] offset:23552\n\t" \
  "ds_write_b128 v8, v[56:59] offset:24576\n\t" \
  "ds_write_b128 v8, v[60:63] offset:25600\n\t" \
  "ds_write_b128 v8, v[64:67] offset:26624\n\t" \
  "ds_write_b128 v8, v[68:71] offset:27648\n\t" \
  "ds_write_b128 v8, v[72:75] offset:28672\n\t" \
  "ds_write_b128 v8, v[76:79] offset:29696\n\t" \
  "ds_write_b128 v8, v[80:83] offset:30720\n\t" \
  "ds_write_b128 v8, v[84:87] offset:31744\n\t" \
  "s_mov_b32 s76, 0\n\t" \
  "s_mov_b32 s77, 0\n\t" \
  "s_mov_b32 s78, 0\n\t" \
  "s_mov_b32 s79, 0\n\t" \
  "s_mov_b32 s80, 0\n\t" \
  "s_mov_b32 s81, 0\n\t" \
  "s_mov_b32 s82, 0\n\t" \
  "s_mov_b32 s83, 0\n\t" \
  "s_mov_b32 s84, 0\n\t" \
  "s_mov_b32 s85, 0\n\t" \
  "s_mov_b32 s86, 0\n\t" \
  "s_mov_b32 s87, 0\n\t" \
  "s_mov_b32 s88, 0\n\t" \
  "s_mov_b32 s89, 0\n\t" \
  "s_mov_b32 s90, 0\n\t" \
  "s_mov_b32 s91, 0\n\t" \
  "s_mov_b32 s92, 0\n\t" \
  "s_lshl_b32 s67, %5, 11\n\t" \
  "s_add_u32 s62, %3, s67\n\t" \
  "s_add_u32 s69, s67, 15872\n\t" \
  "s_mov_b32 m0, s69\n\t" \
  "s_mov_b32 s69, s62\n\t" \
  "s_add_u32 s67, s62, 8192\n\t" \
  "s_mov_b32 s50, 0\n\t" \
  "v_mov_b32 v15, 2110464\n\t" \
  "CH_CAL_%=:\n\t" \
  "v_mov_b32 v14, 2170880\n\t" \
  "buffer_load_dwordx2 v[16:17], v15, s[44:47], 0 offen offset:0 sc1\n\t" \
  "buffer_load_dword v14, v14, s[44:47], 0 offen offset:0 sc1\n\t" \
  "s_waitcnt vmcnt(0)\n\t" \
  "s_nop 0\n\t" \
  "v_readfirstlane_b32 s54, v14\n\t" \
  "v_readfirstlane_b32 s52, v16\n\t" \
  "v_readfirstlane_b32 s51, v17\n\t" \
  "s_nop 3\n\t" \
  "s_add_u32 s54, s54, 1\n\t" \
  "s_xor_b32 s52, s52, s54\n\t" \
  "s_xnor_b32 s51, s51, s54\n\t" \
  "s_andn2_b32 s53, s51, 8192\n\t" \
  "s_or_b32 s52, s52, s53\n\t" \
  "s_cmp_eq_u32 s52, 0\n\t" \
  "s_cbranch_scc1 CH_CALD_%=\n\t" \
  "s_sleep 2\n\t" \
  "s_add_u32 s50, s50, 1\n\t" \
  "s_cmp_lt_u32 s50, 0x40000\n\t" \
  "s_cbranch_scc1 CH_CAL_%=\n\t" \
  "s_branch CH_END_%=\n\t" \
  "CH_CALD_%=:\n\t" \
  "s_and_b32 s51, s51, 8192\n\t" \
  "s_mov_b32 s70, s51\n\t" \
  "s_add_u32 s63, s51, 524288\n\t" \
  "s_mov_b32 s60, -1\n\t" \
  "s_mov_b32 s66, 1\n\t" \
  "s_mov_b32 s48, 0\n\t" \
  "s_mov_b32 s49, 2\n\t" \
  "s_xor_b32 s51, s51, 8192\n\t" \
  "s_add_u32 s58, s44, s51\n\t" \
  "s_addc_u32 s59, s45, 0\n\t" \
  "s_cmp_eq_u32 %5, 2\n\t" \
  "s_cbranch_scc0 CH_PK_%=\n\t" \
  "s_mov_b32 s50, 0\n\t" \
  "CH_KVL1_%=:\n\t" \
  "s_cmp_ge_u32 s48, s49\n\t" \
  "s_cbranch_scc1 CH_KVD1_%=\n\t" \
  "s_sleep 1\n\t" \
  "buffer_load_dword v14, v99, s[44:47], 0 offen offset:0 sc1\n\t" \
  "s_waitcnt vmcnt(0)\n\t" \
  "s_nop 1\n\t" \
  "v_min_i32_dpp v14, v14, v14 row_ror:2 row_mask:0xf bank_mask:0xf\n\t" \
  "s_nop 1\n\t" \
  "v_min_i32_dpp v14, v14, v14 row_ror:1 row_mask:0xf bank_mask:0xf\n\t" \
  "s_nop 1\n\t" \
  "v_readfirstlane_b32 s48, v14\n\t" \
  "s_nop 3\n\t" \
  "s_add_u32 s48, s48, 1\n\t" \
  "s_add_u32 s50, s50, 1\n\t" \
  "s_cmp_lt_u32 s50, 0x40000\n\t" \
  "s_cbranch_scc1 CH_KVL1_%=\n\t" \
  "s_branch CH_END_%=\n\t" \
  "CH_KVD1_%=:\n\t" \
  "CH_PK_%=:\n\t" \
  "s_barrier\n\t" \
  "buffer_load_dwordx4 v7, s[44:47], s62 offen offset:0 sc1 lds\n\t" \
  "buffer_load_dwordx4 v7, s[44:47], s62 offen offset:1024 sc1 lds\n\t" \
  "buffer_load_dwordx4 v[108:111], v7, s[44:47], s67 offen offset:0 sc1\n\t" \
  "buffer_load_dwordx4 v[112:115], v7, s[44:47], s67 offen offset:1024 sc1\n\t" \
  "v_mov_b32 v116, 0x3f803f80\n\t" \
  "v_mov_b32 v117, 0x3f803f80\n\t" \
  "v_mov_b32 v118, 0x3f803f80\n\t" \
  "v_mov_b32 v119, 0x3f803f80\n\t" \
  "v_mov_b32 v120, 0x3f803f80\n\t" \
  "v_mov_b32 v121, 0x3f803f80\n\t" \
  "v_mov_b32 v122, 0x3f803f80\n\t" \
  "v_mov_b32 v123, 0x3f803f80\n\t" \
  "v_mov_b32 v124, -1\n\t" \
  "v_mov_b32 v125, -1\n\t" \
  "v_mov_b32 v126, -1\n\t" \
  "v_mov_b32 v127, -1\n\t" \
  "s_add_u32 s65, %8, 258048\n\t" \
  "buffer_store_dwordx2 v[124:125], v107, s[44:47], s65 offen offset:0 sc1\n\t" \
  "v_mov_b32 v14, 0\n\t" \
  "v_mov_b32 v15, 8448\n\t" \
  "ds_write_b32 v15, v14 offset:0\n\t" \
  "s_waitcnt vmcnt(0)\n\t" \
  "ds_read_b128 v[128:131], v8 offset:0\n\t" \
  "ds_read_b128 v[132:135], v8 offset:1024\n\t" \
  "ds_read_b128 v[136:139], v8 offset:2048\n\t" \
  "ds_read_b128 v[140:143], v8 offset:3072\n\t" \
  "ds_read_b128 v[144:147], v8 offset:4096\n\t" \
  "ds_read_b128 v[148:151], v8 offset:5120\n\t" \
  "ds_read_b128 v[152:155], v8 offset:6144\n\t" \
  "ds_read_b128 v[156:159], v8 offset:7168\n\t" \
  "ds_read_b128 v[160:163], v8 offset:8192\n\t" \
  "ds_read_b128 v[164:167], v8 offset:9216\n\t" \
  "ds_read_b128 v[168:171], v8 offset:10240\n\t" \
  "ds_read_b128 v[172:175], v8 offset:11264\n\t" \
  "ds_read_b128 v[176:179], v8 offset:12288\n\t" \
  "ds_read_b128 v[180:183], v8 offset:13312\n\t" \
  "ds_read_b128 v[184:187], v8 offset:14336\n\t" \
  "ds_read_b128 v[188:191], v8 offset:15360\n\t" \
  "v_mov_b64 v[56:57], 0\n\t" \
  "v_mov_b64 v[58:59], 0\n\t" \
  "v_mov_b64 v[60:61], 0\n\t" \
  "v_mov_b64 v[62:63], 0\n\t" \
  "v_mov_b64 v[64:65], 0\n\t" \
  "v_mov_b64 v[66:67], 0\n\t" \
  "v_mov_b64 v[68:69], 0\n\t" \
  "v_mov_b64 v[70:71], 0\n\t" \
  "v_mov_b64 v[72:73], 0\n\t" \
  "v_mov_b64 v[74:75], 0\n\t" \
  "v_mov_b64 v[76:77], 0\n\t" \
  "v_mov_b64 v[78:79], 0\n\t" \
  "v_mov_b64 v[80:81], 0\n\t" \
  "v_mov_b64 v[82:83], 0\n\t" \
  "v_mov_b64 v[84:85], 0\n\t" \
  "v_mov_b64 v[86:87], 0\n\t" \
  "v_mov_b64 v[88:89], 0\n\t" \
  "v_mov_b64 v[90:91], 0\n\t" \
  "s_waitcnt lgkmcnt(0)\n\t" \
  "s_memrealtime s[52:53]\n\t" \
  "s_waitcnt lgkmcnt(0)\n\t" \
  "s_sub_u32 s36, s52, 2000\n\t" \
  "s_mov_b32 s56, s36\n\t" \
  "s_mov_b32 s57, s36\n\t" \
  "s_mov_b32 s37, 136\n\t" \
  "s_mov_b32 s90, 42\n\t" \
  "s_mov_b32 s38, 1000\n\t" \
  "s_mov_b32 s39, 1000\n\t" \
  "s_or_b32 s50, %9, 2\n\t" \
  "s_cmp_eq_u32 s50, %5\n\t" \
  "s_cselect_b32 s93, 1, 0\n\t" \
  "s_and_b32 s50, %9, 63\n\t" \
  "s_or_b32 s50, s50, %5\n\t" \
  "s_cmp_eq_u32 s50, 20\n\t" \
  "s_cselect_b32 s93, 2, s93\n\t" \
  "s_mov_b32 s50, 1000\n\t" \
  "CH_LOOP_%=:\n\t" \
  "s_barrier\n\t" \
  "buffer_load_dwordx4 v[24:27], v11, s[44:47], s70 offen offset:0 sc1\n\t" \
  "buffer_load_dwordx4 v[28:31], v11, s[44:47], s70 offen offset:1024 sc1\n\t" \
  "s_waitcnt lgkmcnt(0)\n\t" \
  "s_mov_b32 s42, s50\n\t" \
  "s_sub_u32 s53, s52, s56\n\t" \
  "s_cmp_eq_u32 s66, 0\n\t" \
  "s_cselect_b32 s43, s53, 1000\n\t" \
  "s_add_u32 s58, s58, 16384\n\t" \
  "s_addc_u32 s59, s59, 0\n\t" \
  "s_mov_b32 s61, s70\n\t" \
  "s_add_u32 s95, s94, s63\n\t" \
  "s_xor_b32 s63, s63, 524288\n\t" \
  "s_add_u32 s60, s60, 1\n\t" \
  "s_mov_b32 s66, 0\n\t" \
  "s_add_u32 s67, s60, 1\n\t" \
  "s_and_b32 s67, s67, 63\n\t" \
  "s_lshl_b32 s67, s67, 14\n\t" \
  "s_add_u32 s62, s69, s67\n\t" \
  "s_add_u32 s67, s62, 8192\n\t" \
  "ds_read_b128 v[16:19], v10 offset:0\n\t" \
  "buffer_store_dwordx4 v[124:127], v7, s[44:47], s95 offen offset:0 sc1\n\t" \
  "s_sub_u32 s50, s60, 1\n\t" \
  "v_mov_b32 v14, s50\n\t" \
  "buffer_store_dword v14, v103, s[44:47], 0 offen offset:0 sc1\n\t" \
  "s_cmp_eq_u32 s93, 2\n\t" \
  "s_cbranch_scc1 CH_REP_%=\n\t" \
  "CH_REPBACK_%=:\n\t" \
  "s_waitcnt vmcnt(4)\n\t" \
  "v_mov_b64 v[92:93], v[108:109]\n\t" \
  "v_mov_b64 v[96:97], v[110:111]\n\t" \
  "v_mov_b64 v[100:101], v[112:113]\n\t" \
  "v_mov_b64 v[104:105], v[114:115]\n\t" \
  "s_waitcnt vmcnt(3)\n\t" \
  "CH_GOT0_%=:\n\t" \
  "s_waitcnt lgkmcnt(0)\n\t" \
  "ds_write_b128 v16, v[24:27] offset:0\n\t" \
  "v_smfmac_f32_16x16x64_bf16 v[64:67], v[24:27], a[0:7], v0\n\t" \
  "v_max3_u32 v14, v24, v25, v26\n\t" \
  "v_smfmac_f32_16x16x64_bf16 v[68:71], v[24:27], a[8:15], v0\n\t" \
  "s_waitcnt vmcnt(2)\n\t" \
  "CH_GOT1_%=:\n\t" \
  "ds_write_b128 v16, v[28:31] offset:1024\n\t" \
  "v_max3_u32 v14, v14, v27, v28\n\t" \
  "v_max3_u32 v15, v29, v30, v31\n\t" \
  "v_max_u32 v14, v14, v15\n\t" \
  "v_cmp_eq_u32 vcc, -1, v14\n\t" \
  "v_smfmac_f32_16x16x64_bf16 v[56:59], v[24:27], v[128:135], v0\n\t" \
  "v_smfmac_f32_16x16x64_bf16 v[60:63], v[24:27], v[136:143], v0\n\t" \
  "s_cbranch_vccnz CH_RETRYA_%=\n\t" \
  "s_waitcnt lgkmcnt(0)\n\t" \
  "s_barrier\n\t" \
  "ds_read_b128 v[32:35], v17 offset:0\n\t" \
  "s_memrealtime s[52:53]\n\t" \
  "v_smfmac_f32_16x16x64_bf16 v[56:59], v[28:31], v[144:151], v0\n\t" \
  "ds_read_b128 v[36:39], v17 offset:1024\n\t" \
  "v_smfmac_f32_16x16x64_bf16 v[60:63], v[28:31], v[152:159], v0\n\t" \
  "ds_read_b128 v[40:43], v18 offset:0\n\t" \
  "v_smfmac_f32_16x16x64_bf16 v[64:67], v[28:31], a[16:23], v0\n\t" \
  "ds_read_b128 v[44:47], v18 offset:1024\n\t" \
  "v_smfmac_f32_16x16x64_bf16 v[68:71], v[28:31], a[24:31], v0\n\t" \
  "ds_read_b128 v[48:51], v19 offset:0\n\t" \
  "v_smfmac_f32_16x16x64_bf16 v[72:75], v[24:27], a[128:135], v0\n\t" \
  "ds_read_b128 v[52:55], v19 offset:1024\n\t" \
  "v_smfmac_f32_16x16x64_bf16 v[76:79], v[24:27], a[136:143], v0\n\t" \
  "ds_read_b128 v[128:131], v7 offset:15872\n\t" \
  "v_smfmac_f32_16x16x64_bf16 v[72:75], v[28:31], a[144:151], v0\n\t" \
  "ds_read_b128 v[144:147], v7 offset:19968\n\t" \
  "v_smfmac_f32_16x16x64_bf16 v[76:79], v[28:31], a[152:159], v0\n\t" \
  "ds_read_b128 v[132:135], v7 offset:16896\n\t" \
  "s_waitcnt lgkmcnt(8)\n\t" \
  "v_smfmac_f32_16x16x64_bf16 v[56:59], v[32:35], v[160:167], v0\n\t" \
  "ds_read_b128 v[148:151], v7 offset:20992\n\t" \
  "v_smfmac_f32_16x16x64_bf16 v[60:63], v[32:35], v[168:175], v0\n\t" \
  "ds_read_b128 v[136:139], v7 offset:17920\n\t" \
  "s_waitcnt lgkmcnt(9)\n\t" \
  "v_smfmac_f32_16x16x64_bf16 v[56:59], v[36:39], v[176:183], v0\n\t" \
  "ds_read_b128 v[152:155], v7 offset:22016\n\t" \
  "v_smfmac_f32_16x16x64_bf16 v[60:63], v[36:39], v[184:191], v0\n\t" \
  "ds_read_b128 v[140:143], v7 offset:18944\n\t" \
  "s_waitcnt lgkmcnt(10)\n\t" \
  "v_smfmac_f32_16x16x64_bf16 v[56:59], v[40:43], v[192:199], v0\n\t" \
  "ds_read_b128 v[156:159], v7 offset:23040\n\t" \
  "v_smfmac_f32_16x16x64_bf16 v[60:63], v[40:43], v[200:207], v0\n\t" \
  "s_cmp_eq_u32 %5, 2\n\t" \
  "s_cbranch_scc0 CH_NP1_%=\n\t" \
  "buffer_load_dword v28, v99, s[44:47], 0 offen offset:0 sc1\n\t" \
  "CH_NP1_%=:\n\t" \
  "s_waitcnt lgkmcnt(10)\n\t" \
  "v_smfmac_f32_16x16x64_bf16 v[56:59], v[44:47], v[208:215], v0\n\t" \
  "v_smfmac_f32_16x16x64_bf16 v[60:63], v[44:47], v[216:223], v0\n\t" \
  "s_waitcnt lgkmcnt(9)\n\t" \
  "v_smfmac_f32_16x16x64_bf16 v[56:59], v[48:51], v[224:231], v0\n\t" \
  "v_smfmac_f32_16x16x64_bf16 v[60:63], v[48:51], v[232:239], v0\n\t" \
  "buffer_load_dwordx4 v[108:111], v7, s[44:47], s67 offen offset:0 sc1\n\t" \
  "s_waitcnt lgkmcnt(8)\n\t" \
  "v_smfmac_f32_16x16x64_bf16 v[56:59], v[52:55], v[240:247], v0\n\t" \
  "v_smfmac_f32_16x16x64_bf16 v[60:63], v[52:55], v[248:255], v0\n\t" \
  "v_smfmac_f32_16x16x64_bf16 v[64:67], v[32:35], a[32:39], v0\n\t" \
  "s_nop 2\n\t" \
  "v_smfmac_f32_16x16x64_bf16 v[68:71], v[32:35], a[40:47], v0\n\t" \
  "s_nop 1\n\t" \
  "v_add_f32 v16, v56, v58\n\t" \
  "v_add_f32 v17, v57, v59\n\t" \
  "v_add_f32 v18, v60, v62\n\t" \
  "v_add_f32 v19, v61, v63\n\t" \
  "v_cvt_pk_bf16_f32 v16, v16, v18\n\t" \
  "v_cvt_pk_bf16_f32 v17, v17, v19\n\t" \
  "ds_write2_b32 v1, v16, v17 offset0:0 offset1:80\n\t" \
  "v_smfmac_f32_16x16x64_bf16 v[64:67], v[36:39], a[48:55], v0\n\t" \
  "v_smfmac_f32_16x16x64_bf16 v[68:71], v[36:39], a[56:63], v0\n\t" \
  "buffer_load_dwordx4 v[112:115], v7, s[44:47], s67 offen offset:1024 sc1\n\t" \
  "v_smfmac_f32_16x16x64_bf16 v[64:67], v[40:43], a[64:71], v0\n\t" \
  "v_smfmac_f32_16x16x64_bf16 v[68:71], v[40:43], a[72:79], v0\n\t" \
  "v_smfmac_f32_16x16x64_bf16 v[64:67], v[44:47], a[80:87], v0\n\t" \
  "s_waitcnt lgkmcnt(0)\n\t" \
  "s_barrier\n\t" \
  "ds_read_b128 v[160:163], v4 offset:0\n\t" \
  "v_smfmac_f32_16x16x64_bf16 v[68:71], v[44:47], a[88:95], v0\n\t" \
  "ds_read_b128 v[164:167], v4 offset:64\n\t" \
  "s_cmp_eq_u32 s93, 1\n\t" \
  "s_cbranch_scc1 CH_LATE_%=\n\t" \
  "CH_LATEBACK_%=:\n\t" \
  "v_smfmac_f32_16x16x64_bf16 v[64:67], v[48:51], a[96:103], v0\n\t" \
  "ds_read_b128 v[168:171], v4 offset:128\n\t" \
  "v_smfmac_f32_16x16x64_bf16 v[68:71], v[48:51], a[104:111], v0\n\t" \
  "ds_read_b128 v[172:175], v4 offset:192\n\t" \
  "v_smfmac_f32_16x16x64_bf16 v[64:67], v[52:55], a[112:119], v0\n\t" \
  "buffer_load_dwordx4 v7, s[44:47], s62 offen offset:0 sc1 lds\n\t" \
  "v_smfmac_f32_16x16x64_bf16 v[68:71], v[52:55], a[120:127], v0\n\t" \
  "s_waitcnt lgkmcnt(3)\n\t" \
  "v_mfma_f32_16x16x32_bf16 v[56:59], v[128:131], v[160:163], 0\n\t" \
  "s_nop 1\n\t" \
  "v_mfma_f32_16x16x32_bf16 v[60:63], v[144:147], v[160:163], 0\n\t" \
  "s_nop 1\n\t" \
  "v_add_f32 v16, v64, v66\n\t" \
  "s_waitcnt lgkmcnt(2)\n\t" \
  "v_mfma_f32_16x16x32_bf16 v[56:59], v[132:135], v[164:167], v[56:59]\n\t" \
  "v_add_f32 v17, v65, v67\n\t" \
  "v_mfma_f32_16x16x32_bf16 v[60:63], v[148:151], v[164:167], v[60:63]\n\t" \
  "v_add_f32 v18, v68, v70\n\t" \
  "s_waitcnt lgkmcnt(1)\n\t" \
  "v_mfma_f32_16x16x32_bf16 v[56:59], v[136:139], v[168:171], v[56:59]\n\t" \
  "v_add_f32 v19, v69, v71\n\t" \
  "v_mfma_f32_16x16x32_bf16 v[60:63], v[152:155], v[168:171], v[60:63]\n\t" \
  "v_cvt_pk_bf16_f32 v12, v16, v18\n\t" \
  "s_waitcnt lgkmcnt(0)\n\t" \
  "v_mfma_f32_16x16x32_bf16 v[56:59], v[140:143], v[172:175], v[56:59]\n\t" \
  "v_cvt_pk_bf16_f32 v13, v17, v19\n\t" \
  "v_mfma_f32_16x16x32_bf16 v[60:63], v[156:159], v[172:175], v[60:63]\n\t" \
  "ds_write2_b32 v2, v12, v13 offset0:0 offset1:80\n\t" \
  "v_smfmac_f32_16x16x64_bf16 v[72:75], v[32:35], a[160:167], v0\n\t" \
  "buffer_load_dwordx4 v7, s[44:47], s62 offen offset:1024 sc1 lds\n\t" \
  "v_smfmac_f32_16x16x64_bf16 v[76:79], v[32:35], a[168:175], v0\n\t" \
  "v_smfmac_f32_16x16x64_bf16 v[72:75], v[36:39], a[176:183], v0\n\t" \
  "v_smfmac_f32_16x16x64_bf16 v[76:79], v[36:39], a[184:191], v0\n\t" \
  "s_waitcnt lgkmcnt(0)\n\t" \
  "s_barrier\n\t" \
  "ds_read_b128 v[176:179], v5 offset:0\n\t" \
  "v_smfmac_f32_16x16x64_bf16 v[72:75], v[40:43], a[192:199], v0\n\t" \
  "ds_read_b128 v[180:183], v5 offset:64\n\t" \
  "v_max3_f32 v15, v56, v57, v58\n\t" \
  "v_smfmac_f32_16x16x64_bf16 v[76:79], v[40:43], a[200:207], v0\n\t" \
  "ds_read_b128 v[184:187], v5 offset:128\n\t" \
  "v_max3_f32 v15, v15, v59, v60\n\t" \
  "v_smfmac_f32_16x16x64_bf16 v[72:75], v[44:47], a[208:215], v0\n\t" \
  "ds_read_b128 v[188:191], v5 offset:192\n\t" \
  "v_max3_f32 v15, v15, v61, v62\n\t" \
  "v_smfmac_f32_16x16x64_bf16 v[76:79], v[44:47], a[216:223], v0\n\t" \
  "ds_read_b128 v[128:131], v8 offset:16384\n\t" \
  "v_max_f32 v15, v15, v63\n\t" \
  "v_smfmac_f32_16x16x64_bf16 v[72:75], v[48:51], a[224:231], v0\n\t" \
  "ds_read_b128 v[132:135], v8 offset:17408\n\t" \
  "v_mov_b32 v14, v15\n\t" \
  "v_smfmac_f32_16x16x64_bf16 v[76:79], v[48:51], a[232:239], v0\n\t" \
  "ds_read_b128 v[136:139], v8 offset:18432\n\t" \
  "s_nop 0\n\t" \
  "v_permlane16_swap_b32 v15, v14\n\t" \
  "s_waitcnt lgkmcnt(6)\n\t" \
  "v_mfma_f32_16x16x32_bf16 v[64:67], v[176:179], v[160:163], 0\n\t" \
  "ds_read_b128 v[140:143], v8 offset:19456\n\t" \
  "v_cndmask_b32 v20, v56, v58, s[72:73]\n\t" \
  "s_waitcnt lgkmcnt(6)\n\t" \
  "v_mfma_f32_16x16x32_bf16 v[64:67], v[180:183], v[164:167], v[64:67]\n\t" \
  "ds_read_b128 v[144:147], v8 offset:20480\n\t" \
  "v_cndmask_b32 v21, v57, v59, s[72:73]\n\t" \
  "s_waitcnt lgkmcnt(6)\n\t" \
  "v_mfma_f32_16x16x32_bf16 v[64:67], v[184:187], v[168:171], v[64:67]\n\t" \
  "ds_read_b128 v[148:151], v8 offset:21504\n\t" \
  "v_cndmask_b32 v22, v60, v62, s[72:73]\n\t" \
  "s_waitcnt lgkmcnt(6)\n\t" \
  "v_mfma_f32_16x16x32_bf16 v[64:67], v[188:191], v[172:175], v[64:67]\n\t" \
  "ds_read_b128 v[152:155], v8 offset:22528\n\t" \
  "v_cndmask_b32 v23, v61, v63, s[72:73]\n\t" \
  "v_smfmac_f32_16x16x64_bf16 v[72:75], v[52:55], a[240:247], v0\n\t" \
  "ds_read_b128 v[156:159], v8 offset:23552\n\t" \
  "s_nop 0\n\t" \
  "v_smfmac_f32_16x16x64_bf16 v[76:79], v[52:55], a[248:255], v0\n\t" \
  "ds_read_b128 v[160:163], v8 offset:24576\n\t" \
  "s_nop 1\n\t" \
  "v_max3_f32 v15, v15, v14, v64\n\t" \
  "v_max3_f32 v15, v15, v65, v66\n\t" \
  "v_max_f32 v15, v15, v67\n\t" \
  "v_mov_b32 v14, v15\n\t" \
  "v_cndmask_b32 v16, v64, v66, s[72:73]\n\t" \
  "ds_read_b128 v[164:167], v8 offset:25600\n\t" \
  "v_permlane32_swap_b32 v15, v14\n\t" \
  "v_max_f32 v15, v15, v14\n\t" \
  "v_mul_f32 v15, s68, v15\n\t" \
  "v_fma_f32 v16, v16, s68, -v15\n\t" \
  "v_fma_f32 v20, v20, s68, -v15\n\t" \
  "v_fma_f32 v21, v21, s68, -v15\n\t" \
  "v_fma_f32 v22, v22, s68, -v15\n\t" \
  "v_fma_f32 v23, v23, s68, -v15\n\t" \
  "v_exp_f32 v16, v16\n\t" \
  "v_exp_f32 v20, v20\n\t" \
  "v_exp_f32 v21, v21\n\t" \
  "v_exp_f32 v22, v22\n\t" \
  "v_exp_f32 v23, v23\n\t" \
  "v_cvt_pk_bf16_f32 v14, v16, 0\n\t" \
  "v_cvt_pk_bf16_f32 v68, v20, v21\n\t" \
  "v_cvt_pk_bf16_f32 v70, v22, v23\n\t" \
  "v_cndmask_b32 v69, v14, 0, s[72:73]\n\t" \
  "v_cndmask_b32 v71, 0, v14, s[72:73]\n\t" \
  "v_pk_add_f32 v[16:17], v[72:73], v[74:75]\n\t" \
  "v_pk_add_f32 v[18:19], v[76:77], v[78:79]\n\t" \
  "v_smfmac_f32_16x16x64_bf16 v[80:83], v[68:71], v[116:123], v0 abid:1\n\t" \
  "v_cvt_pk_bf16_f32 v94, v16, v17\n\t" \
  "v_cvt_pk_bf16_f32 v98, v17, v16\n\t" \
  "v_cvt_pk_bf16_f32 v102, v18, v19\n\t" \
  "v_cvt_pk_bf16_f32 v106, v19, v18\n\t" \
  "v_smfmac_f32_16x16x64_bf16 v[84:87], v[68:71], v[92:99], v0 abid:1\n\t" \
  "ds_read_b128 v[168:171], v8 offset:26624\n\t" \
  "v_smfmac_f32_16x16x64_bf16 v[88:91], v[68:71], v[100:107], v0 abid:1\n\t" \
  "ds_read_b128 v[172:175], v8 offset:27648\n\t" \
  "v_pk_add_f32 v[16:17], v[80:81], v[82:83]\n\t" \
  "v_rcp_f32 v16, v16\n\t" \
  "v_rcp_f32 v17, v17\n\t" \
  "v_mov_b64 v[40:41], 0\n\t" \
  "v_mov_b64 v[42:43], 0\n\t" \
  "v_pk_add_f32 v[18:19], v[84:85], v[86:87]\n\t" \
  "v_pk_mul_f32 v[18:19], v[18:19], v[16:17]\n\t" \
  "v_pk_add_f32 v[20:21], v[88:89], v[90:91]\n\t" \
  "v_pk_mul_f32 v[20:21], v[20:21], v[16:17]\n\t" \
  "v_cvt_pk_bf16_f32 v18, v18, v20\n\t" \
  "v_cvt_pk_bf16_f32 v19, v19, v21\n\t" \
  "ds_write2_b32 v3, v18, v19 offset0:0 offset1:80\n\t" \
  "v_mov_b64 v[44:45], 0\n\t" \
  "v_mov_b64 v[46:47], 0\n\t" \
  "v_mov_b64 v[48:49], 0\n\t" \
  "v_mov_b64 v[50:51], 0\n\t" \
  "v_mov_b64 v[52:53], 0\n\t" \
  "v_mov_b64 v[54:55], 0\n\t" \
  "s_waitcnt vmcnt(5)\n\t" \
  "s_waitcnt lgkmcnt(0)\n\t" \
  "s_barrier\n\t" \
  "ds_read_b128 v[56:59], v6 offset:0\n\t" \
  "ds_read_b128 v[60:63], v6 offset:128\n\t" \
  "ds_read_b128 v[176:179], v8 offset:28672\n\t" \
  "ds_read_b128 v[180:183], v8 offset:29696\n\t" \
  "ds_read_b128 v[184:187], v8 offset:30720\n\t" \
  "ds_read_b128 v[188:191], v8 offset:31744\n\t" \
  "s_mul_i32 s65, s60, 4096\n\t" \
  "s_add_u32 s65, s65, %8\n\t" \
  "s_add_u32 s70, s61, 16384\n\t" \
  "s_add_u32 s64, %4, s63\n\t" \
  "s_add_u32 s49, s60, 3\n\t" \
  "s_min_u32 s49, s49, 64\n\t" \
  "s_cmp_lt_u32 %5, 1\n\t" \
  "s_waitcnt lgkmcnt(5)\n\t" \
  "v_smfmac_f32_16x16x64_bf16 v[40:43], v[56:59], v[128:135], v0\n\t" \
  "s_waitcnt lgkmcnt(4)\n\t" \
  "v_smfmac_f32_16x16x64_bf16 v[40:43], v[60:63], v[136:143], v0\n\t" \
  "v_smfmac_f32_16x16x64_bf16 v[44:47], v[56:59], v[144:151], v0\n\t" \
  "v_smfmac_f32_16x16x64_bf16 v[44:47], v[60:63], v[152:159], v0\n\t" \
  "v_smfmac_f32_16x16x64_bf16 v[48:51], v[56:59], v[160:167], v0\n\t" \
  "v_smfmac_f32_16x16x64_bf16 v[48:51], v[60:63], v[168:175], v0\n\t" \
  "s_waitcnt lgkmcnt(2)\n\t" \
  "v_smfmac_f32_16x16x64_bf16 v[52:55], v[56:59], v[176:183], v0\n\t" \
  "s_waitcnt lgkmcnt(0)\n\t" \
  "v_smfmac_f32_16x16x64_bf16 v[52:55], v[60:63], v[184:191], v0\n\t" \
  "v_pk_add_f32 v[16:17], v[40:41], v[42:43]\n\t" \
  "v_cvt_pk_bf16_f32 v24, v16, v17\n\t" \
  "v_min_u32 v24, -2, v24\n\t" \
  "v_pk_add_f32 v[18:19], v[44:45], v[46:47]\n\t" \
  "v_cvt_pk_bf16_f32 v25, v18, v19\n\t" \
  "v_min_u32 v25, -2, v25\n\t" \
  "v_pk_add_f32 v[20:21], v[48:49], v[50:51]\n\t" \
  "v_cvt_pk_bf16_f32 v26, v20, v21\n\t" \
  "v_min_u32 v26, -2, v26\n\t" \
  "v_pk_add_f32 v[22:23], v[52:53], v[54:55]\n\t" \
  "v_cvt_pk_bf16_f32 v27, v22, v23\n\t" \
  "v_min_u32 v27, -2, v27\n\t" \
  "buffer_store_dwordx4 v[24:27], v9, s[44:47], s63 offen offset:0 sc1\n\t" \
  "v_cndmask_b32 v12, v12, v94, s[74:75]\n\t" \
  "v_cndmask_b32 v13, v13, v102, s[74:75]\n\t" \
  "buffer_store_dwordx2 v[12:13], v107, s[44:47], s65 offen offset:0 sc1\n\t" \
  "s_cbranch_scc1 CH_RED_%=\n\t" \
  "s_load_dwordx2 s[56:57], s[58:59], 0\n\t" \
  "s_cmp_eq_u32 s93, 1\n\t" \
  "s_cbranch_scc0 CH_NL_%=\n\t" \
  "s_load_dwordx2 s[38:39], s[58:59], 128\n\t" \
  "s_load_dwordx2 s[40:41], s[58:59], 256\n\t" \
  "s_load_dwordx2 s[96:97], s[58:59], 384\n\t" \
  "s_load_dwordx2 s[98:99], s[58:59], 512\n\t" \
  "CH_NL_%=:\n\t" \
  "ds_read_b128 v[128:131], v8 offset:0\n\t" \
  "ds_read_b128 v[132:135], v8 offset:1024\n\t" \
  "ds_read_b128 v[136:139], v8 offset:2048\n\t" \
  "ds_read_b128 v[140:143], v8 offset:3072\n\t" \
  "ds_read_b128 v[144:147], v8 offset:4096\n\t" \
  "ds_read_b128 v[148:151], v8 offset:5120\n\t" \
  "ds_read_b128 v[152:155], v8 offset:6144\n\t" \
  "ds_read_b128 v[156:159], v8 offset:7168\n\t" \
  "ds_read_b128 v[160:163], v8 offset:8192\n\t" \
  "ds_read_b128 v[164:167], v8 offset:9216\n\t" \
  "ds_read_b128 v[168:171], v8 offset:10240\n\t" \
  "ds_read_b128 v[172:175], v8 offset:11264\n\t" \
  "ds_read_b128 v[176:179], v8 offset:12288\n\t" \
  "ds_read_b128 v[180:183], v8 offset:13312\n\t" \
  "ds_read_b128 v[184:187], v8 offset:14336\n\t" \
  "ds_read_b128 v[188:191], v8 offset:15360\n\t" \
  "v_mov_b64 v[56:57], 0\n\t" \
  "v_mov_b64 v[58:59], 0\n\t" \
  "v_mov_b64 v[60:61], 0\n\t" \
  "v_mov_b64 v[62:63], 0\n\t" \
  "v_mov_b64 v[64:65], 0\n\t" \
  "v_mov_b64 v[66:67], 0\n\t" \
  "v_mov_b64 v[68:69], 0\n\t" \
  "v_mov_b64 v[70:71], 0\n\t" \
  "v_mov_b64 v[72:73], 0\n\t" \
  "v_mov_b64 v[74:75], 0\n\t" \
  "v_mov_b64 v[76:77], 0\n\t" \
  "v_mov_b64 v[78:79], 0\n\t" \
  "v_mov_b64 v[80:81], 0\n\t" \
  "v_mov_b64 v[82:83], 0\n\t" \
  "v_mov_b64 v[84:85], 0\n\t" \
  "v_mov_b64 v[86:87], 0\n\t" \
  "v_mov_b64 v[88:89], 0\n\t" \
  "v_mov_b64 v[90:91], 0\n\t" \
  "s_cmp_eq_u32 %5, 2\n\t" \
  "s_cbranch_scc0 CH_N2_%=\n\t" \
  "s_waitcnt vmcnt(2)\n\t" \
  "s_nop 1\n\t" \
  "v_min_i32_dpp v28, v28, v28 row_ror:2 row_mask:0xf bank_mask:0xf\n\t" \
  "s_nop 1\n\t" \
  "v_min_i32_dpp v28, v28, v28 row_ror:1 row_mask:0xf bank_mask:0xf\n\t" \
  "s_nop 1\n\t" \
  "v_readfirstlane_b32 s48, v28\n\t" \
  "s_nop 3\n\t" \
  "s_add_u32 s48, s48, 1\n\t" \
  "s_mov_b32 s50, 0\n\t" \
  "CH_KVL2_%=:\n\t" \
  "s_cmp_ge_u32 s48, s49\n\t" \
  "s_cbranch_scc1 CH_KVD2_%=\n\t" \
  "s_sleep 1\n\t" \
  "buffer_load_dword v14, v99, s[44:47], 0 offen offset:0 sc1\n\t" \
  "s_waitcnt vmcnt(0)\n\t" \
  "s_nop 1\n\t" \
  "v_min_i32_dpp v14, v14, v14 row_ror:2 row_mask:0xf bank_mask:0xf\n\t" \
  "s_nop 1\n\t" \
  "v_min_i32_dpp v14, v14, v14 row_ror:1 row_mask:0xf bank_mask:0xf\n\t" \
  "s_nop 1\n\t" \
  "v_readfirstlane_b32 s48, v14\n\t" \
  "s_nop 3\n\t" \
  "s_add_u32 s48, s48, 1\n\t" \
  "s_add_u32 s50, s50, 1\n\t" \
  "s_cmp_lt_u32 s50, 0x40000\n\t" \
  "s_cbranch_scc1 CH_KVL2_%=\n\t" \
  "s_branch CH_END_%=\n\t" \
  "CH_KVD2_%=:\n\t" \
  "CH_N2_%=:\n\t" \
  "s_waitcnt vmcnt(0)\n\t" \
  "s_cmp_eq_u32 %5, 3\n\t" \
  "s_cbranch_scc0 CH_JOIN_%=\n\t" \
  "s_waitcnt lgkmcnt(0)\n\t" \
  "s_mov_b32 s51, s56\n\t" \
  "s_memrealtime s[52:53]\n\t" \
  "s_waitcnt lgkmcnt(0)\n\t" \
  "s_sub_u32 s53, s52, s51\n\t" \
  "s_cmp_lt_i32 s53, -2000\n\t" \
  "s_cbranch_scc1 CH_WD1_%=\n\t" \
  "s_cmp_ge_i32 s53, -3\n\t" \
  "s_cbranch_scc1 CH_WC1_%=\n\t" \
  "CH_WF1_%=:\n\t" \
  "s_memrealtime s[52:53]\n\t" \
  "s_waitcnt lgkmcnt(0)\n\t" \
  "s_sub_u32 s53, s52, s51\n\t" \
  "s_cmp_lt_i32 s53, -3\n\t" \
  "s_cbranch_scc1 CH_WF1_%=\n\t" \
  "s_min_i32 s53, s53, 0\n\t" \
  "s_mul_i32 s52, s53, 12\n\t" \
  "s_add_u32 s52, s52, 48\n\t" \
  "s_getpc_b64 s[54:55]\n\t" \
  "s_add_u32 s54, s54, s52\n\t" \
  "s_addc_u32 s55, s55, 0\n\t" \
  "s_setpc_b64 s[54:55]\n\t" \
  "s_nop 1\n\t" \
  "s_nop 1\n\t" \
  "s_nop 1\n\t" \
  "s_nop 1\n\t" \
  "s_nop 1\n\t" \
  "s_nop 1\n\t" \
  "s_nop 1\n\t" \
  "s_nop 1\n\t" \
  "s_nop 1\n\t" \
  "s_branch CH_WD1_%=\n\t" \
  "CH_WC1_%=:\n\t" \
  "s_cmp_ge_i32 s53, 0\n\t" \
  "s_cbranch_scc1 CH_WD1_%=\n\t" \
  "s_memrealtime s[52:53]\n\t" \
  "s_waitcnt lgkmcnt(0)\n\t" \
  "s_sub_u32 s53, s52, s51\n\t" \
  "s_branch CH_WC1_%=\n\t" \
  "CH_WD1_%=:\n\t" \
  "s_branch CH_JOIN_%=\n\t" \
  "CH_RED_%=:\n\t" \
  "ds_read_b128 v[32:35], v10 offset:1024\n\t" \
  "s_mov_b32 s66, 0\n\t" \
  "s_mov_b32 s51, s57\n\t" \
  "s_memrealtime s[52:53]\n\t" \
  "s_waitcnt lgkmcnt(0)\n\t" \
  "s_sub_u32 s53, s52, s51\n\t" \
  "s_mov_b32 s50, s53\n\t" \
  "s_cmp_lt_i32 s53, -2000\n\t" \
  "s_cbranch_scc1 CH_WD2_%=\n\t" \
  "s_cmp_ge_i32 s53, -3\n\t" \
  "s_cbranch_scc1 CH_WC2_%=\n\t" \
  "CH_WF2_%=:\n\t" \
  "s_memrealtime s[52:53]\n\t" \
  "s_waitcnt lgkmcnt(0)\n\t" \
  "s_sub_u32 s53, s52, s51\n\t" \
  "s_cmp_lt_i32 s53, -3\n\t" \
  "s_cbranch_scc1 CH_WF2_%=\n\t" \
  "s_min_i32 s53, s53, 0\n\t" \
  "s_mul_i32 s52, s53, 12\n\t" \
  "s_add_u32 s52, s52, 48\n\t" \
  "s_getpc_b64 s[54:55]\n\t" \
  "s_add_u32 s54, s54, s52\n\t" \
  "s_addc_u32 s55, s55, 0\n\t" \
  "s_setpc_b64 s[54:55]\n\t" \
  "s_nop 1\n\t" \
  "s_nop 1\n\t" \
  "s_nop 1\n\t" \
  "s_nop 1\n\t" \
  "s_nop 1\n\t" \
  "s_nop 1\n\t" \
  "s_nop 1\n\t" \
  "s_nop 1\n\t" \
  "s_nop 1\n\t" \
  "s_branch CH_WD2_%=\n\t" \
  "CH_WC2_%=:\n\t" \
  "s_cmp_ge_i32 s53, 0\n\t" \
  "s_cbranch_scc1 CH_WD2_%=\n\t" \
  "s_memrealtime s[52:53]\n\t" \
  "s_waitcnt lgkmcnt(0)\n\t" \
  "s_sub_u32 s53, s52, s51\n\t" \
  "s_branch CH_WC2_%=\n\t" \
  "CH_WD2_%=:\n\t" \
  "s_add_u32 s52, s50, 2000\n\t" \
  "s_cmp_le_u32 s52, 2008\n\t" \
  "s_cbranch_scc1 CH_GOB_%=\n\t" \
  "s_nop 15\n\t" \
  "s_nop 15\n\t" \
  "s_nop 15\n\t" \
  "s_nop 15\n\t" \
  "s_nop 15\n\t" \
  "s_nop 15\n\t" \
  "s_nop 15\n\t" \
  "s_nop 15\n\t" \
  "s_nop 1\n\t" \
  "CH_GOB_%=:\n\t" \
  "buffer_load_dwordx4 v[40:43], v99, s[44:47], s64 offen offset:0 sc1\n\t" \
  "buffer_load_dwordx4 v[44:47], v99, s[44:47], s64 offen offset:1024 sc1\n\t" \
  "buffer_load_dwordx4 v[48:51], v99, s[44:47], s64 offen offset:2048 sc1\n\t" \
  "buffer_load_dwordx4 v[52:55], v99, s[44:47], s64 offen offset:3072 sc1\n\t" \
  "s_load_dwordx2 s[56:57], s[58:59], 0\n\t" \
  "ds_read_b128 v[128:131], v8 offset:0\n\t" \
  "ds_read_b128 v[132:135], v8 offset:1024\n\t" \
  "ds_read_b128 v[136:139], v8 offset:2048\n\t" \
  "ds_read_b128 v[140:143], v8 offset:3072\n\t" \
  "ds_read_b128 v[144:147], v8 offset:4096\n\t" \
  "ds_read_b128 v[148:151], v8 offset:5120\n\t" \
  "ds_read_b128 v[152:155], v8 offset:6144\n\t" \
  "ds_read_b128 v[156:159], v8 offset:7168\n\t" \
  "ds_read_b128 v[160:163], v8 offset:8192\n\t" \
  "ds_read_b128 v[164:167], v8 offset:9216\n\t" \
  "ds_read_b128 v[168:171], v8 offset:10240\n\t" \
  "ds_read_b128 v[172:175], v8 offset:11264\n\t" \
  "ds_read_b128 v[176:179], v8 offset:12288\n\t" \
  "ds_read_b128 v[180:183], v8 offset:13312\n\t" \
  "ds_read_b128 v[184:187], v8 offset:14336\n\t" \
  "ds_read_b128 v[188:191], v8 offset:15360\n\t" \
  "v_mov_b64 v[56:57], 0\n\t" \
  "v_mov_b64 v[58:59], 0\n\t" \
  "v_mov_b64 v[60:61], 0\n\t" \
  "v_mov_b64 v[62:63], 0\n\t" \
  "v_mov_b64 v[64:65], 0\n\t" \
  "v_mov_b64 v[66:67], 0\n\t" \
  "v_mov_b64 v[68:69], 0\n\t" \
  "v_mov_b64 v[70:71], 0\n\t" \
  "v_mov_b64 v[72:73], 0\n\t" \
  "v_mov_b64 v[74:75], 0\n\t" \
  "v_mov_b64 v[76:77], 0\n\t" \
  "v_mov_b64 v[78:79], 0\n\t" \
  "v_mov_b64 v[80:81], 0\n\t" \
  "v_mov_b64 v[82:83], 0\n\t" \
  "v_mov_b64 v[84:85], 0\n\t" \
  "v_mov_b64 v[86:87], 0\n\t" \
  "v_mov_b64 v[88:89], 0\n\t" \
  "v_mov_b64 v[90:91], 0\n\t" \
  "CH_CHKB_%=:\n\t" \
  "s_waitcnt vmcnt(3)\n\t" \
  "v_mfma_f32_16x16x32_bf16 v[28:31], v[32:35], v[40:43], 0\n\t" \
  "v_max3_u32 v14, v40, v41, v42\n\t" \
  "v_max_u32 v14, v14, v43\n\t" \
  "s_waitcnt vmcnt(2)\n\t" \
  "v_mfma_f32_16x16x32_bf16 v[28:31], v[32:35], v[44:47], v[28:31]\n\t" \
  "v_max3_u32 v14, v14, v44, v45\n\t" \
  "v_max3_u32 v14, v14, v46, v47\n\t" \
  "s_waitcnt vmcnt(1)\n\t" \
  "v_mfma_f32_16x16x32_bf16 v[28:31], v[32:35], v[48:51], v[28:31]\n\t" \
  "v_max3_u32 v14, v14, v48, v49\n\t" \
  "v_max3_u32 v14, v14, v50, v51\n\t" \
  "s_waitcnt vmcnt(0)\n\t" \
  "v_mfma_f32_16x16x32_bf16 v[28:31], v[32:35], v[52:55], v[28:31]\n\t" \
  "v_max3_u32 v14, v14, v52, v53\n\t" \
  "v_max3_u32 v14, v14, v54, v55\n\t" \
  "v_cmp_eq_u32 vcc, -1, v14\n\t" \
  "s_nop 4\n\t" \
  "v_add_f32_dpp v16, v28, v28 row_ror:8 row_mask:0xf bank_mask:0xf\n\t" \
  "v_add_f32_dpp v17, v29, v29 row_ror:8 row_mask:0xf bank_mask:0xf\n\t" \
  "v_add_f32_dpp v18, v30, v30 row_ror:8 row_mask:0xf bank_mask:0xf\n\t" \
  "v_add_f32_dpp v19, v31, v31 row_ror:8 row_mask:0xf bank_mask:0xf\n\t" \
  "v_cvt_pk_bf16_f32 v22, v16, v17\n\t" \
  "v_cvt_pk_bf16_f32 v23, v18, v19\n\t" \
  "v_min_u32 v22, -2, v22\n\t" \
  "v_min_u32 v23, -2, v23\n\t" \
  "s_cbranch_vccnz CH_RETRYB_%=\n\t" \
  "buffer_store_dwordx2 v[22:23], v95, s[44:47], s70 offen offset:0 sc1\n\t" \
  "s_memrealtime s[52:53]\n\t" \
  "CH_JOIN_%=:\n\t" \
  "s_cmp_lt_u32 s60, 62\n\t" \
  "s_cbranch_scc1 CH_LOOP_%=\n\t" \
  "s_nop 3\n\t" \
  "s_barrier\n\t" \
  "buffer_load_dwordx4 v[24:27], v11, s[44:47], s70 offen offset:0 sc1\n\t" \
  "buffer_load_dwordx4 v[28:31], v11, s[44:47], s70 offen offset:1024 sc1\n\t" \
  "s_waitcnt lgkmcnt(0)\n\t" \
  "s_mov_b32 s42, s50\n\t" \
  "s_sub_u32 s53, s52, s56\n\t" \
  "s_cmp_eq_u32 s66, 0\n\t" \
  "s_cselect_b32 s43, s53, 1000\n\t" \
  "s_add_u32 s58, s58, 16384\n\t" \
  "s_addc_u32 s59, s59, 0\n\t" \
  "s_mov_b32 s61, s70\n\t" \
  "s_add_u32 s95, s94, s63\n\t" \
  "s_xor_b32 s63, s63, 524288\n\t" \
  "s_add_u32 s60, s60, 1\n\t" \
  "s_mov_b32 s66, 0\n\t" \
  "s_add_u32 s67, s60, 1\n\t" \
  "s_and_b32 s67, s67, 63\n\t" \
  "s_lshl_b32 s67, s67, 14\n\t" \
  "s_add_u32 s62, s69, s67\n\t" \
  "s_add_u32 s67, s62, 8192\n\t" \
  "ds_read_b128 v[16:19], v10 offset:0\n\t" \
  "buffer_store_dwordx4 v[124:127], v7, s[44:47], s95 offen offset:0 sc1\n\t" \
  "s_sub_u32 s50, s60, 1\n\t" \
  "v_mov_b32 v14, s50\n\t" \
  "buffer_store_dword v14, v103, s[44:47], 0 offen offset:0 sc1\n\t" \
  "s_waitcnt vmcnt(4)\n\t" \
  "v_mov_b64 v[92:93], v[108:109]\n\t" \
  "v_mov_b64 v[96:97], v[110:111]\n\t" \
  "v_mov_b64 v[100:101], v[112:113]\n\t" \
  "v_mov_b64 v[104:105], v[114:115]\n\t" \
  "s_waitcnt vmcnt(3)\n\t" \
  "CH_GOT0F_%=:\n\t" \
  "s_waitcnt lgkmcnt(0)\n\t" \
  "ds_write_b128 v16, v[24:27] offset:0\n\t" \
  "v_smfmac_f32_16x16x64_bf16 v[64:67], v[24:27], a[0:7], v0\n\t" \
  "v_max3_u32 v14, v24, v25, v26\n\t" \
  "v_smfmac_f32_16x16x64_bf16 v[68:71], v[24:27], a[8:15], v0\n\t" \
  "s_waitcnt vmcnt(2)\n\t" \
  "CH_GOT1F_%=:\n\t" \
  "ds_write_b128 v16, v[28:31] offset:1024\n\t" \
  "v_max3_u32 v14, v14, v27, v28\n\t" \
  "v_max3_u32 v15, v29, v30, v31\n\t" \
  "v_max_u32 v14, v14, v15\n\t" \
  "v_cmp_eq_u32 vcc, -1, v14\n\t" \
  "v_smfmac_f32_16x16x64_bf16 v[72:75], v[24:27], a[128:135], v0\n\t" \
  "v_smfmac_f32_16x16x64_bf16 v[76:79], v[24:27], a[136:143], v0\n\t" \
  "s_cbranch_vccnz CH_RETRYAF_%=\n\t" \
  "s_waitcnt lgkmcnt(0)\n\t" \
  "s_barrier\n\t" \
  "ds_read_b128 v[32:35], v17 offset:0\n\t" \
  "v_mov_b32 v14, 63\n\t" \
  "v_smfmac_f32_16x16x64_bf16 v[64:67], v[28:31], a[16:23], v0\n\t" \
  "ds_read_b128 v[36:39], v17 offset:1024\n\t" \
  "v_smfmac_f32_16x16x64_bf16 v[68:71], v[28:31], a[24:31], v0\n\t" \
  "ds_read_b128 v[40:43], v18 offset:0\n\t" \
  "v_smfmac_f32_16x16x64_bf16 v[72:75], v[28:31], a[144:151], v0\n\t" \
  "ds_read_b128 v[44:47], v18 offset:1024\n\t" \
  "v_smfmac_f32_16x16x64_bf16 v[76:79], v[28:31], a[152:159], v0\n\t" \
  "ds_read_b128 v[48:51], v19 offset:0\n\t" \
  "ds_read_b128 v[52:55], v19 offset:1024\n\t" \
  "buffer_store_dword v14, v103, s[44:47], 0 offen offset:0 sc1\n\t" \
  "s_waitcnt lgkmcnt(5)\n\t" \
  "v_smfmac_f32_16x16x64_bf16 v[64:67], v[32:35], a[32:39], v0\n\t" \
  "v_smfmac_f32_16x16x64_bf16 v[68:71], v[32:35], a[40:47], v0\n\t" \
  "s_waitcnt lgkmcnt(4)\n\t" \
  "v_smfmac_f32_16x16x64_bf16 v[64:67], v[36:39], a[48:55], v0\n\t" \
  "v_smfmac_f32_16x16x64_bf16 v[68:71], v[36:39], a[56:63], v0\n\t" \
  "s_waitcnt lgkmcnt(3)\n\t" \
  "v_smfmac_f32_16x16x64_bf16 v[64:67], v[40:43], a[64:71], v0\n\t" \
  "v_smfmac_f32_16x16x64_bf16 v[68:71], v[40:43], a[72:79], v0\n\t" \
  "s_waitcnt lgkmcnt(2)\n\t" \
  "v_smfmac_f32_16x16x64_bf16 v[64:67], v[44:47], a[80:87], v0\n\t" \
  "v_smfmac_f32_16x16x64_bf16 v[68:71], v[44:47], a[88:95], v0\n\t" \
  "s_waitcnt lgkmcnt(1)\n\t" \
  "v_smfmac_f32_16x16x64_bf16 v[64:67], v[48:51], a[96:103], v0\n\t" \
  "v_smfmac_f32_16x16x64_bf16 v[68:71], v[48:51], a[104:111], v0\n\t" \
  "s_waitcnt lgkmcnt(0)\n\t" \
  "v_smfmac_f32_16x16x64_bf16 v[64:67], v[52:55], a[112:119], v0\n\t" \
  "v_smfmac_f32_16x16x64_bf16 v[68:71], v[52:55], a[120:127], v0\n\t" \
  "v_smfmac_f32_16x16x64_bf16 v[72:75], v[32:35], a[160:167], v0\n\t" \
  "v_smfmac_f32_16x16x64_bf16 v[76:79], v[32:35], a[168:175], v0\n\t" \
  "v_smfmac_f32_16x16x64_bf16 v[72:75], v[36:39], a[176:183], v0\n\t" \
  "v_smfmac_f32_16x16x64_bf16 v[76:79], v[36:39], a[184:191], v0\n\t" \
  "v_smfmac_f32_16x16x64_bf16 v[72:75], v[40:43], a[192:199], v0\n\t" \
  "v_smfmac_f32_16x16x64_bf16 v[76:79], v[40:43], a[200:207], v0\n\t" \
  "v_smfmac_f32_16x16x64_bf16 v[72:75], v[44:47], a[208:215], v0\n\t" \
  "v_smfmac_f32_16x16x64_bf16 v[76:79], v[44:47], a[216:223], v0\n\t" \
  "v_smfmac_f32_16x16x64_bf16 v[72:75], v[48:51], a[224:231], v0\n\t" \
  "v_smfmac_f32_16x16x64_bf16 v[76:79], v[48:51], a[232:239], v0\n\t" \
  "v_smfmac_f32_16x16x64_bf16 v[72:75], v[52:55], a[240:247], v0\n\t" \
  "v_smfmac_f32_16x16x64_bf16 v[76:79], v[52:55], a[248:255], v0\n\t" \
  "v_pk_add_f32 v[16:17], v[64:65], v[66:67]\n\t" \
  "v_pk_add_f32 v[18:19], v[68:69], v[70:71]\n\t" \
  "v_cvt_pk_bf16_f32 v12, v16, v18\n\t" \
  "v_cvt_pk_bf16_f32 v13, v17, v19\n\t" \
  "s_nop 2\n\t" \
  "v_pk_add_f32 v[16:17], v[72:73], v[74:75]\n\t" \
  "v_pk_add_f32 v[18:19], v[76:77], v[78:79]\n\t" \
  "v_cvt_pk_bf16_f32 v14, v16, v17\n\t" \
  "v_cvt_pk_bf16_f32 v15, v18, v19\n\t" \
  "v_cndmask_b32 v12, v12, v14, s[74:75]\n\t" \
  "v_cndmask_b32 v13, v13, v15, s[74:75]\n\t" \
  "s_add_u32 s65, %8, 258048\n\t" \
  "v_min_u32 v12, -2, v12\n\t" \
  "v_min_u32 v13, -2, v13\n\t" \
  "buffer_store_dwordx2 v[12:13], v107, s[44:47], s65 offen offset:0 sc1\n\t" \
  "s_branch CH_END_%=\n\t" \
  "CH_RETRYA_%=:\n\t" \
  "s_add_u32 s66, s66, 1\n\t" \
  "s_cmp_lt_u32 s66, 0x40000\n\t" \
  "s_cbranch_scc0 CH_END_%=\n\t" \
  "s_nop 3\n\t" \
  "buffer_load_dwordx4 v[24:27], v11, s[44:47], s61 offen offset:0 sc1\n\t" \
  "buffer_load_dwordx4 v[28:31], v11, s[44:47], s61 offen offset:1024 sc1\n\t" \
  "s_nop 7\n\t" \
  "v_mov_b64 v[56:57], 0\n\t" \
  "v_mov_b64 v[58:59], 0\n\t" \
  "v_mov_b64 v[60:61], 0\n\t" \
  "v_mov_b64 v[62:63], 0\n\t" \
  "v_mov_b64 v[64:65], 0\n\t" \
  "v_mov_b64 v[66:67], 0\n\t" \
  "v_mov_b64 v[68:69], 0\n\t" \
  "v_mov_b64 v[70:71], 0\n\t" \
  "v_mov_b64 v[72:73], 0\n\t" \
  "v_mov_b64 v[74:75], 0\n\t" \
  "v_mov_b64 v[76:77], 0\n\t" \
  "v_mov_b64 v[78:79], 0\n\t" \
  "s_waitcnt vmcnt(0)\n\t" \
  "s_branch CH_GOT0_%=\n\t" \
  "CH_RETRYAF_%=:\n\t" \
  "s_add_u32 s66, s66, 1\n\t" \
  "s_cmp_lt_u32 s66, 0x40000\n\t" \
  "s_cbranch_scc0 CH_END_%=\n\t" \
  "s_nop 3\n\t" \
  "buffer_load_dwordx4 v[24:27], v11, s[44:47], s61 offen offset:0 sc1\n\t" \
  "buffer_load_dwordx4 v[28:31], v11, s[44:47], s61 offen offset:1024 sc1\n\t" \
  "s_nop 7\n\t" \
  "v_mov_b64 v[56:57], 0\n\t" \
  "v_mov_b64 v[58:59], 0\n\t" \
  "v_mov_b64 v[60:61], 0\n\t" \
  "v_mov_b64 v[62:63], 0\n\t" \
  "v_mov_b64 v[64:65], 0\n\t" \
  "v_mov_b64 v[66:67], 0\n\t" \
  "v_mov_b64 v[68:69], 0\n\t" \
  "v_mov_b64 v[70:71], 0\n\t" \
  "v_mov_b64 v[72:73], 0\n\t" \
  "v_mov_b64 v[74:75], 0\n\t" \
  "v_mov_b64 v[76:77], 0\n\t" \
  "v_mov_b64 v[78:79], 0\n\t" \
  "s_waitcnt vmcnt(0)\n\t" \
  "s_branch CH_GOT0F_%=\n\t" \
  "CH_LATE_%=:\n\t" \
  "s_max_i32 s38, s38, s40\n\t" \
  "s_max_i32 s96, s96, s98\n\t" \
  "s_max_i32 s38, s38, s96\n\t" \
  "s_add_u32 s53, s38, 6\n\t" \
  "s_cmp_lt_u32 s53, 14\n\t" \
  "s_addc_u32 s37, s37, 0\n\t" \
  "s_add_u32 s53, s38, 47\n\t" \
  "s_cmp_lt_u32 s53, 39\n\t" \
  "s_subb_u32 s37, s37, 0\n\t" \
  "s_max_i32 s39, s39, s41\n\t" \
  "s_max_i32 s97, s97, s99\n\t" \
  "s_max_i32 s39, s39, s97\n\t" \
  "s_add_u32 s53, s39, 3\n\t" \
  "s_cmp_lt_u32 s53, 14\n\t" \
  "s_addc_u32 s90, s90, 0\n\t" \
  "s_add_u32 s53, s39, 44\n\t" \
  "s_cmp_lt_u32 s53, 39\n\t" \
  "s_subb_u32 s90, s90, 0\n\t" \
  "s_sub_u32 s50, s52, 56\n\t" \
  "s_sub_u32 s53, s50, s36\n\t" \
  "s_add_u32 s50, s50, 45\n\t" \
  "s_add_u32 s50, s50, s37\n\t" \
  "s_cmp_gt_i32 s53, 0\n\t" \
  "s_cselect_b32 s50, s50, s57\n\t" \
  "s_add_u32 s36, s50, s90\n\t" \
  "s_add_u32 s50, s36, s37\n\t" \
  "v_mov_b32 v14, s36\n\t" \
  "v_mov_b32 v15, s50\n\t" \
  "s_xor_b32 s52, s61, 8192\n\t" \
  "v_mov_b32 v23, 16384\n\t" \
  "s_mov_b64 exec, 1\n\t" \
  "buffer_store_dwordx2 v[14:15], v23, s[44:47], s52 offen offset:0 sc1\n\t" \
  "s_mov_b64 exec, -1\n\t" \
  "s_nop 3\n\t" \
  "s_branch CH_LATEBACK_%=\n\t" \
  "CH_REP_%=:\n\t" \
  "v_mov_b32 v20, s42\n\t" \
  "v_mov_b32 v21, s43\n\t" \
  "s_xor_b32 s52, s61, 8192\n\t" \
  "s_lshl_b32 s53, %9, 1\n\t" \
  "s_add_u32 s53, s53, 16384 + 128 - 40\n\t" \
  "v_mov_b32 v23, s53\n\t" \
  "s_waitcnt vmcnt(2)\n\t" \
  "s_mov_b64 exec, 1\n\t" \
  "buffer_store_dwordx2 v[20:21], v23, s[44:47], s52 offen offset:0 sc1\n\t" \
  "s_mov_b64 exec, -1\n\t" \
  "v_mov_b64 v[92:93], v[108:109]\n\t" \
  "v_mov_b64 v[96:97], v[110:111]\n\t" \
  "v_mov_b64 v[100:101], v[112:113]\n\t" \
  "v_mov_b64 v[104:105], v[114:115]\n\t" \
  "s_waitcnt lgkmcnt(0)\n\t" \
  "ds_write_b128 v16, v[24:27] offset:0\n\t" \
  "v_smfmac_f32_16x16x64_bf16 v[64:67], v[24:27], a[0:7], v0\n\t" \
  "v_max3_u32 v14, v24, v25, v26\n\t" \
  "v_smfmac_f32_16x16x64_bf16 v[68:71], v[24:27], a[8:15], v0\n\t" \
  "s_branch CH_GOT1_%=\n\t" \
  "CH_RETRYB_%=:\n\t" \
  "s_add_u32 s66, s66, 1\n\t" \
  "s_cmp_lt_u32 s66, 0x40000\n\t" \
  "s_cbranch_scc0 CH_END_%=\n\t" \
  "s_nop 3\n\t" \
  "buffer_load_dwordx4 v[40:43], v99, s[44:47], s64 offen offset:0 sc1\n\t" \
  "buffer_load_dwordx4 v[44:47], v99, s[44:47], s64 offen offset:1024 sc1\n\t" \
  "buffer_load_dwordx4 v[48:51], v99, s[44:47], s64 offen offset:2048 sc1\n\t" \
  "buffer_load_dwordx4 v[52:55], v99, s[44:47], s64 offen offset:3072 sc1\n\t" \
  "s_branch CH_CHKB_%=\n\t" \
  "CH_END_%=:\n\t" \
  "s_waitcnt vmcnt(0)\n\t" \
  "s_waitcnt lgkmcnt(0)\n\t" \
  "s_mov_b32 m0, s71\n\t" \
  ""
#define CHAIN_ASM_CLOBBERS "v0", "v1", "v2", "v3", "v4", "v5", "v6", "v7", "v8", "v9", "v10", "v11", "v12", "v13", "v14", "v15", "v16", "v17", "v18", "v19", "v20", "v21", "v22", "v23", "v24", "v25", "v26", "v27", "v28", "v29", "v30", "v31", "v32", "v33", "v34", "v35", "v36", "v37", "v38", "v39", "v40", "v41", "v42", "v43", "v44", "v45", "v46", "v47", "v48", "v49", "v50", "v51", "v52", "v53", "v54", "v55", "v56", "v57", "v58", "v59", "v60", "v61", "v62", "v63", "v64", "v65", "v66", "v67", "v68", "v69", "v70", "v71", "v72", "v73", "v74", "v75", "v76", "v77", "v78", "v79", "v80", "v81", "v82", "v83", "v84", "v85", "v86", "v87", "v88", "v89", "v90", "v91", "v92", "v93", "v94", "v95", "v96", "v97", "v98", "v99", "v100", "v101", "v102", "v103", "v104", "v105", "v106", "v107", "v108", "v109", "v110", "v111", "v112", "v113", "v114", "v115", "v116", "v117", "v118", "v119", "v120", "v121", "v122", "v123", "v124", "v125", "v126", "v127", "v128", "v129", "v130", "v131", "v132", "v133", "v134", "v135", "v136", "v137", "v138", "v139", "v140", "v141", "v142", "v143", "v144", "v145", "v146", "v147", "v148", "v149", "v150", "v151", "v152", "v153", "v154", "v155", "v156", "v157", "v158", "v159", "v160", "v161", "v162", "v163", "v164", "v165", "v166", "v167", "v168", "v169", "v170", "v171", "v172", "v173", "v174", "v175", "v176", "v177", "v178", "v179", "v180", "v181", "v182", "v183", "v184", "v185", "v186", "v187", "v188", "v189", "v190", "v191", "v192", "v193", "v194", "v195", "v196", "v197", "v198", "v199", "v200", "v201", "v202", "v203", "v204", "v205", "v206", "v207", "v208", "v209", "v210", "v211", "v212", "v213", "v214", "v215", "v216", "v217", "v218", "v219", "v220", "v221", "v222", "v223", "v224", "v225", "v226", "v227", "v228", "v229", "v230", "v231", "v232", "v233", "v234", "v235", "v236", "v237", "v238", "v239", "v240", "v241", "v242", "v243", "v244", "v245", "v246", "v247", "v248", "v249", "v250", "v251", "v252", "v253", "v254", "v255", "a0", "a1", "a2", "a3", "a4", "a5", "a6", "a7", "a8", "a9", "a10", "a11", "a12", "a13", "a14", "a15", "a16", "a17", "a18", "a19", "a20", "a21", "a22", "a23", "a24", "a25", "a26", "a27", "a28", "a29", "a30", "a31", "a32", "a33", "a34", "a35", "a36", "a37", "a38", "a39", "a40", "a41", "a42", "a43", "a44", "a45", "a46", "a47", "a48", "a49", "a50", "a51", "a52", "a53", "a54", "a55", "a56", "a57", "a58", "a59", "a60", "a61", "a62", "a63", "a64", "a65", "a66", "a67", "a68", "a69", "a70", "a71", "a72", "a73", "a74", "a75", "a76", "a77", "a78", "a79", "a80", "a81", "a82", "a83", "a84", "a85", "a86", "a87", "a88", "a89", "a90", "a91", "a92", "a93", "a94", "a95", "a96", "a97", "a98", "a99", "a100", "a101", "a102", "a103", "a104", "a105", "a106", "a107", "a108", "a109", "a110", "a111", "a112", "a113", "a114", "a115", "a116", "a117", "a118", "a119", "a120", "a121", "a122", "a123", "a124", "a125", "a126", "a127", "a128", "a129", "a130", "a131", "a132", "a133", "a134", "a135", "a136", "a137", "a138", "a139", "a140", "a141", "a142", "a143", "a144", "a145", "a146", "a147", "a148", "a149", "a150", "a151", "a152", "a153", "a154", "a155", "a156", "a157", "a158", "a159", "a160", "a161", "a162", "a163", "a164", "a165", "a166", "a167", "a168", "a169", "a170", "a171", "a172", "a173", "a174", "a175", "a176", "a177", "a178", "a179", "a180", "a181", "a182", "a183", "a184", "a185", "a186", "a187", "a188", "a189", "a190", "a191", "a192", "a193", "a194", "a195", "a196", "a197", "a198", "a199", "a200", "a201", "a202", "a203", "a204", "a205", "a206", "a207", "a208", "a209", "a210", "a211", "a212", "a213", "a214", "a215", "a216", "a217", "a218", "a219", "a220", "a221", "a222", "a223", "a224", "a225", "a226", "a227", "a228", "a229", "a230", "a231", "a232", "a233", "a234", "a235", "a236", "a237", "a238", "a239", "a240", "a241", "a242", "a243", "a244", "a245", "a246", "a247", "a248", "a249", "a250", "a251", "a252", "a253", "a254", "a255", "s36", "s37", "s38", "s39", "s40", "s41", "s42", "s43", "s44", "s45", "s46", "s47", "s48", "s49", "s50", "s51", "s52", "s53", "s54", "s55", "s56", "s57", "s58", "s59", "s60", "s61", "s62", "s63", "s64", "s65", "s66", "s67", "s68", "s69", "s70", "s71", "s72", "s73", "s74", "s75", "s76", "s77", "s78", "s79", "s80", "s81", "s82", "s83", "s84", "s85", "s86", "s87", "s88", "s89", "s90", "s91", "s92", "s93", "s94", "s95", "s96", "s97", "s98", "s99", "vcc", "scc", "memory"
constexpr int L_TOTAL = CH_L_WL + 4 * 32768;
DI void chain_role(const Params& p, unsigned char* lds, int rank) {
    unsigned char* ws = p.ws;
    const int h = rank >> 1, half = rank & 1;
    const int tid = threadIdx.x, lane = tid & 63, w = __builtin_amdgcn_readfirstlane(tid >> 6), g = lane >> 4, n = lane & 15, par = (lane >> 1) & 1;
    const int tok = 2 * (n >> 2) + (n & 1);
    const u32 coff = WS_MAIL;
    static_assert(C_CAL == CH_CAL_OFF && WS_EP == CH_EP_OFF, "");
    u32* cst = (u32*)(lds + CH_L_WL + w * 32768) + lane;
    const int gq = (n >> 2) ^ (n & 1); const int mu = (gq < 2 ? 4 * gq : 4 * (gq - 2) + 1) + 2 * ((n >> 1) & 1);
    const int rdst = (8 * half + 2 * w + (n >> 3)) * 4 + g;
    const u32 fw = CH_L_F + 2 * g * CH_FS + (16 * w + n) * 4;
    cst[0 * 64] = par ? 0x4E4EEEEEu : 0x44444444u;
    cst[1 * 64] = fw;
    cst[2 * 64] = fw + 8 * CH_FS;
    cst[3 * 64] = fw + 16 * CH_FS;
    cst[4 * 64] = CH_L_F + tok * CH_FS + g * 16;
    cst[5 * 64] = CH_L_F + (8 + mu) * CH_FS + g * 16;
    cst[6 * 64] = CH_L_F + (16 + tok) * CH_FS + g * 32 + par * 16;
    cst[7 * 64] = lane * 16;
    cst[8 * 64] = CH_L_WL + w * 32768 + lane * 16;
    cst[9 * 64] = MB + coff + (u32)(rdst >> 1) * 16384u + (u32)(rdst & 1) * 4096u + (u32)h * 128u + (u32)(n & 7) * 16u;
    cst[10 * 64] = CH_L_CT + w * 2048 + lane * 16;
    cst[11 * 64] = coff + 2 * w * 1024 + (g >> 1) * 512 + (n >> 2) * 128 + (g & 1) * 64 + (n & 3) * 16;
    if (CH_RW == 1) cst[12 * 64] = (g < 2 && n < 8) ? coff + rank * 128 + (n >> 2) * 64 + ((n >> 1) & 1) * 32 + g * 16 + (n & 1) * 8 : 0x80000000u;
    else cst[12 * 64] = (g < 2 && n < 4) ? coff + rank * 128 + (w & 1) * 64 + (n >> 1) * 32 + g * 16 + (n & 1) * 8 : 0x80000000u;
    cst[13 * 64] = w == 2 ? C_P1 + h * 128 + (lane & 3) * 4 : (CH_RW == 1 ? lane * 16 : (lane >> 2) * 128 + (w & 1) * 64 + (lane & 3) * 16);
    cst[14 * 64] = tid == 192 ? C_CP + rank * 128 : 0x80000000u;
    cst[15 * 64] = lane * 8;
    { const int qq = n >> 2;
      const int rq = 4 * (g >> 1) + (g & 1) + 16 * (qq & 1) + 2 * (qq >> 1), ro = 8 * (g >> 1) + 2 * (g & 1) + 16 * (qq & 1) + 4 * (qq >> 1);
      cst[16 * 64] = (4 * w + g) * 8192 + h * 256 + (n ^ (g << 1)) * 16;
      cst[17 * 64] = (h * 128 + 2 * w + (lane >> 5)) * 1024 + half * 512 + ((lane & 31) ^ (w << 1)) * 16;
      cst[18 * 64] = CH_L_WL + rq * 256 + ((2 * w) ^ ((rq & 3) | (((rq >> 4) & 1) << 2))) * 32 + (n & 3) * 8;
      cst[19 * 64] = CH_L_WL + ro * 512 + (w >> 1) * 256 + ((4 * (w & 1)) ^ (((ro >> 1) & 3) | (((ro >> 4) & 1) << 2))) * 32 + (n & 3) * 8;
      cst[20 * 64] = (w * 32 + (lane >> 1)) * 8192 + h * 256 + (lane & 1) * 128; }
    { u32x4 t; for (int k = 0; k < 4; ++k) t[k] = CH_L_MST + ((w + k) & 3) * 2048 + lane * 16; *(u32x4*)(lds + CH_L_CT + w * 2048 + lane * 16) = t;
      for (int k = 0; k < 4; ++k) t[k] = (n & 3) == k ? (((n >> 2) & 1) ? 0x3F800000u : 0x00003F80u) : 0u; *(u32x4*)(lds + CH_L_CT + w * 2048 + 1024 + lane * 16) = t; }
    const u32 sCB = CH_L_WL + w * 32768;
    const u32 sKV0 = WS_KV + (u32)h * RKV * 16384u;
    const u32 sIN0 = MB + coff + (u32)(rank >> 1) * 16384u + (u32)(rank & 1) * 4096u;
    const u32 sDBG = WS_DBG + (u32)(rank * 4 + w) * 256u;
    const u32 sPUB0 = WS_KM + (u32)h * 65u * 4096u + half * 2048 + w * 512;
    const u32 sP1 = rank * 4;
    asm volatile("s_waitcnt lgkmcnt(0)\n\t" CHAIN_ASM_TEXT :: "s"(uni(ws)), "s"(sCB), "s"(uni(p.Wq)), "s"(sKV0), "s"(sIN0), "s"(w), "s"(half), "s"(sDBG), "s"(sPUB0), "s"(sP1), "s"(uni(p.Wk)), "s"(uni(p.Wv)), "s"(uni(p.Wo)) : CHAIN_ASM_CLOBBERS);
}

template <int CTRL> DI float dpp_add(float v) { return v + __uint_as_float(__builtin_amdgcn_update_dpp(0u, __float_as_uint(v), CTRL, 0xf, 0xf, false)); }
DI float swap16_add(float v) { auto r = __builtin_amdgcn_permlane16_swap(__float_as_uint(v), __float_as_uint(v), false, false); return __uint_as_float(r[0]) + __uint_as_float(r[1]); }
DI float swap32_add(float v) { auto r = __builtin_amdgcn_permlane32_swap(__float_as_uint(v), __float_as_uint(v), false, false); return __uint_as_float(r[0]) + __uint_as_float(r[1]); }
DI float swap16_max(float v) { auto r = __builtin_amdgcn_permlane16_swap(__float_as_uint(v), __float_as_uint(v), false, false); return fmaxf(__uint_as_float(r[0]), __uint_as_float(r[1])); }
DI float swap32_max(float v) { auto r = __builtin_amdgcn_permlane32_swap(__float_as_uint(v), __float_as_uint(v), false, false); return fmaxf(__uint_as_float(r[0]), __uint_as_float(r[1])); }
DI float wsum(float v) { v = dpp_add<0x121>(v); v = dpp_add<0x122>(v); v = dpp_add<0x124>(v); v = dpp_add<0x128>(v); return swap32_add(swap16_add(v)); }
template <int NR, bool ROUND, class FN>
DI void mod_part(const Params& p, rsrc_t r, unsigned char* lds, int col0, int row0, u32 dst, u32 tag, FN behind_loads) {
    float* sc = (float*)(lds + 139264); float* red = sc + 512;
    const int tid = threadIdx.x, ch = tid & 3, rg = tid >> 2;
    constexpr int RP = NR / 64;
    u32x4 wv[RP];
#pragma unroll
    for (int i = 0; i < RP; ++i) wv[i] = *(const u32x4*)(p.wm + (size_t)(row0 + rg * RP + i) * 3072 + col0 + ch * 8);
    u16 cv[NR / 256];
#pragma unroll
    for (int i = 0; i < NR / 256; ++i) cv[i] = p.c[row0 + i * 256 + tid];
    SCHED();
    behind_loads();
    SCHED();
#pragma unroll
    for (int i = 0; i < NR / 256; ++i) { const float v = bf2f(cv[i]); sc[i * 256 + tid] = rbf(v / (1.f + __expf(-v))); }
    LBAR();
    float a[8] = {0, 0, 0, 0, 0, 0, 0, 0};
#pragma unroll
    for (int i = 0; i < RP; ++i) { const float s = sc[rg * RP + i];
#pragma unroll
        for (int j = 0; j < 4; ++j) { a[2 * j] += s * blo(wv[i][j]); a[2 * j + 1] += s * bhi(wv[i][j]); } }
#pragma unroll
    for (int j = 0; j < 8; ++j) { a[j] = swap32_add(swap16_add(dpp_add<0x128>(dpp_add<0x124>(a[j])))); }
    if ((tid & 63) < 4) {
#pragma unroll
        for (int j = 0; j < 8; ++j) red[(tid >> 6) * 32 + ch * 8 + j] = a[j]; }
    LBAR();
    if (tid < 32) { float s = (red[tid] + red[32 + tid]) + (red[64 + tid] + red[96 + tid]); if (ROUND) s = rbf(s);
        if (ROUND) st1(r, min(__float_as_uint(s), 0xFFFFFFFEu), dst + tid * 4, 0); else st2(r, __float_as_uint(s), tag, dst + tid * 8); }
}
DI u32x4 get4(rsrc_t r, u32 voff) {
    u32x4 a;
#pragma unroll 1
    for (u32 sp = 0; sp < SPIN_CAP; ++sp) {
        CFENCE();
        a = ld4(r, voff, 0);
        if (!__any(max(max(a.x, a.y), max(a.z, a.w)) == 0xFFFFFFFFu)) break;
        __builtin_amdgcn_s_sleep(2);
    }
    return a;
}
DI f32x4 asf4(u32x4 v) { return __builtin_bit_cast(f32x4, v); }
DI void calib(const Params& p, rsrc_t r, int i, u32 tag) {
    u32 best[2] = {0xFFFFFFFEu, 0xFFFFFFFEu}, acc = 0;
    const u32 base = (40 + i) * 16384; const int lane = threadIdx.x;
    st1(r, 0xFFFFFFFFu, base + (lane & 7) * 2048, 0); DRAIN();
#pragma unroll
    for (int rep = 0; rep < 4; ++rep) {
        unsigned long long c1, c2; asm volatile("s_memtime %0\n\ts_waitcnt lgkmcnt(0)" : "=s"(c1) :: "memory");
        acc += ld1(r, base + (rep & 1) * 8192 + (rep >> 1) * 4096, 0); DRAIN(); asm volatile("" :: "v"(acc));
        asm volatile("s_memtime %0\n\ts_waitcnt lgkmcnt(0)" : "=s"(c2) :: "memory");
        best[rep & 1] = min(best[rep & 1], (u32)(c2 - c1));
    }
    GAS u32* cm = (GAS u32*)(p.ws + C_CALM);
    if (lane < 2) __hip_atomic_fetch_min(cm + lane, lane ? best[1] : best[0], RLX_AGENT);
    DRAIN();
    if (lane == 0) { const u32 n = __hip_atomic_fetch_add(cm + 32, 1u, RLX_AGENT);
        if (n == 14u) { const u32 a = __hip_atomic_load(cm, RLX_AGENT), b = __hip_atomic_load(cm + 1, RLX_AGENT);
            __builtin_amdgcn_raw_buffer_store_b64((u32x2){tag, ~tag ^ (a < b ? 0u : 8192u)}, r, C_CAL, 0, 16); } }
}
template <int MT>
DI void proj_loop(const Params& p, rsrc_t r, unsigned char* lds, const bf16x8 (&W)[4][16], int h, int eh, int tid, u32 tag) {
    constexpr int NS = 64, ST = 1, s0 = 0;
    const int lane = tid & 63, g = lane >> 4, n = lane & 15;
    const u32 flag = C_P1 + h * 128 + (MT * 2 + eh) * 4;
    const u32 ob = (MT == 2 ? WS_Q + h * RKV * 8192 : WS_KV + h * RKV * 16384 + MT * 8192) + (MT == 0 ? ((n >> 2) & 1) * 4096 + (g * 16 + (n >> 3) * 4 + (n & 3)) * 16 : lane * 16);
    const u32 ho = WS_H + tid * 16;
    u32x4 hv[8];
    wait_tag(r, C_HF + (n & 3) * 128 + g * 8, tag);
#pragma unroll
    for (int k = 0; k < 8; ++k) hv[k] = ld4(r, ho + k * 4096, 0);
    SCHED();
#pragma unroll
    for (int k = 0; k < 8; ++k) { *(u32x4*)(lds + k * 4096 + tid * 16) = hv[k]; hv[k] = ld4(r, ho + k * 4096, 32768); }
    LBAR();
#pragma unroll 1
    for (int it = 0; it < NS; ++it) {
        const int seg = s0 + it * ST, nx = it + 2 < NS ? seg + 2 * ST : seg;
        const unsigned char* L = lds + (it & 1) * 32768 + g * 512 + n * 16;
        unsigned char* Ln = lds + ((it + 1) & 1) * 32768 + tid * 16;
        f32x4 acc[2][4];
#pragma unroll
        for (int a = 0; a < 2; ++a)
#pragma unroll
            for (int t = 0; t < 4; ++t) acc[a][t] = (f32x4){0, 0, 0, 0};
        bf16x8 f[16][2];
#pragma unroll
        for (int k = 0; k < 2; ++k) { f[k][0] = *(const bf16x8*)(L + k * 2048); f[k][1] = *(const bf16x8*)(L + k * 2048 + 256); }
#pragma unroll
        for (int k = 0; k < 16; ++k) {
            if (k + 2 < 16) { f[k + 2][0] = *(const bf16x8*)(L + (k + 2) * 2048); f[k + 2][1] = *(const bf16x8*)(L + (k + 2) * 2048 + 256); }
            SCHED();
#pragma unroll
            for (int t = 0; t < 4; ++t) acc[0][t] = MT == 1 ? MFMA(f[k][0], W[t][k], acc[0][t]) : MFMA(W[t][k], f[k][0], acc[0][t]);
            SCHED();
            if (k & 1) { *(u32x4*)(Ln + (k >> 1) * 4096) = hv[k >> 1]; hv[k >> 1] = ld4(r, ho + (k >> 1) * 4096, nx * 32768); }
            if (k == 6) { asm volatile("s_waitcnt vmcnt(3)" ::: "memory");
                if (lane == 0 && it > 1) st1(r, it - 1, flag, 0); }
            SCHED();
#pragma unroll
            for (int t = 0; t < 4; ++t) acc[1][t] = MT == 1 ? MFMA(f[k][1], W[t][k], acc[1][t]) : MFMA(W[t][k], f[k][1], acc[1][t]);
            SCHED();
        }
        const u32 so = KVS(seg) * (MT == 2 ? 8192 : 16384);
        if (MT == 1) {
#pragma unroll
            for (int t = 0; t < 4; ++t) { const u32x4 o = {pk2(acc[0][t][0], acc[0][t][1]), pk2(acc[0][t][2], acc[0][t][3]), pk2(acc[1][t][0], acc[1][t][1]), pk2(acc[1][t][2], acc[1][t][3])};
                st4(r, o, ob + (eh * 4 + t) * 1024, so); }
        } else {
#pragma unroll
            for (int a = 0; a < 2; ++a)
#pragma unroll
                for (int k = 0; k < 2; ++k) { const f32x4 x = acc[a][2 * k], y = acc[a][2 * k + 1]; const u32x4 o = {pk2(x[0], y[0]), pk2(x[1], y[1]), pk2(x[2], y[2]), pk2(x[3], y[3])};
                    st4(r, o, ob + (MT == 0 ? a * 128 : a * 4096) + (eh * 2 + k) * 1024, so); }
        }
        if (it == 0) { wait_tag(r, C_HF + (tid >> 2) * 128 + (tid & 3) * 8, tag); DRAIN(); if (lane == 0) st1(r, 0, flag, 0); RS_(15); }
        LBAR();
    }
    DRAIN(); if (lane == 0) st1(r, 63, flag, 0);
}
DI void load_w(const u16* Wa, const u16* Wb, unsigned char* lds, bf16x8 (&W)[4][16], int rot, int tid) {
    const int lane = tid & 63, w = __builtin_amdgcn_readfirstlane(tid >> 6), g = lane >> 4, n = lane & 15;
    const int q = n >> 2, pp = n & 3;
    const unsigned char* b0[4]; const unsigned char* b1[4];
#pragma unroll
    for (int t = 0; t < 4; ++t) { const u32 ch = ((w & 1) * 4 + t) * 2 + (pp >> 1);
        b0[t] = lds + (w >> 1) * 65536 + imgoff(8 * g + q, ch) + 8 * (pp & 1); b1[t] = lds + (w >> 1) * 65536 + imgoff(8 * g + 4 + q, ch) + 8 * (pp & 1); }
    const u32 lo = (u32)(w * 4 + g) * 8192u + (u32)(n ^ swz(w * 4 + g)) * 16u;
#pragma unroll
    for (int hf = 0; hf < 2; ++hf) {
#pragma unroll 4
        for (int i = 0; i < 32; ++i) { const int ii = (i + rot) & 31; const unsigned char* src = (const unsigned char*)(ii < 16 ? Wa : Wb) + (size_t)(hf * 256 + (ii & 15) * 16) * 8192;
            __builtin_amdgcn_global_load_lds((const GAS u32*)(src + lo), (LAS u32*)(lds + ii * 4096 + w * 1024), 16, 0, 0); }
        DRAIN_DMA(); LBAR();
#pragma unroll
        for (int t = 0; t < 4; ++t)
#pragma unroll
            for (int k = 0; k < 8; ++k) W[t][hf * 8 + k] = trfrag(b0[t] + k * 8192, b1[t] + k * 8192);
#pragma unroll
        for (int t = 0; t < 4; ++t)
#pragma unroll
            for (int k = 0; k < 8; ++k) if (t < 3 || k < 4) PIN_A(W[t][hf * 8 + k]);
        LBAR();
    }
}
DI void proj_role(const Params& p, rsrc_t r, unsigned char* lds, int j, u32 tag) {
    const int tid = threadIdx.x, w = __builtin_amdgcn_readfirstlane(tid >> 6);
    bf16x8 W[4][16];
    const u16* wa = j < 32 ? p.Wk + j * 128 : p.Wq + (j - 32) * 256; const u16* wb = j < 32 ? p.Wv + j * 128 : wa + 128;
    load_w(wa, wb, lds, W, 0, tid);
    wait_init(r, tag, tid & 63);
    RS_(4);
    if (j >= 32) proj_loop<2>(p, r, lds, W, (j - 32) * 2 + (w >> 1), w & 1, tid, tag);
    else if (w < 2) proj_loop<0>(p, r, lds, W, j, w & 1, tid, tag);
    else proj_loop<1>(p, r, lds, W, j, w & 1, tid, tag);
}
DI void ln_role(const Params& p, rsrc_t r, unsigned char* lds, int sg, u32 tag) {
    const int tid = threadIdx.x, lane = tid & 63, w = __builtin_amdgcn_readfirstlane(tid >> 6);
        const int t = lane & 7, cg = lane >> 3, tk = w * 8 + t;
        float* ms = (float*)(lds + 131072);
        u32x4 xv[8];
        mod_part<256, false>(p, r, lds, (sg & 31) * 32, (sg >> 5) * 256, C_MODP + (sg >> 5) * 8192 + (sg & 31) * 256, tag, [&]() {
#pragma unroll
            for (int i = 0; i < 8; ++i) xv[i] = *(const u32x4*)(p.x + (size_t)(sg * 32 + tk) * 512 + (i * 8 + cg) * 8); });
        float s = 0;
#pragma unroll
        for (int i = 0; i < 8; ++i)
#pragma unroll
            for (int k = 0; k < 4; ++k) s += blo(xv[i][k]) + bhi(xv[i][k]);
        s = swap32_add(swap16_add(dpp_add<0x128>(s)));
        const float m = s * (1.f / 512); float q = 0;
#pragma unroll
        for (int i = 0; i < 8; ++i)
#pragma unroll
            for (int k = 0; k < 4; ++k) { const float a = blo(xv[i][k]) - m, b = bhi(xv[i][k]) - m; q += a * a + b * b; }
        q = swap32_add(swap16_add(dpp_add<0x128>(q)));
        const float rs = rsqrtf(q * (1.f / 512) + 1e-5f);
        f32x2 xn[8][4];
#pragma unroll
        for (int i = 0; i < 8; ++i)
#pragma unroll
            for (int k = 0; k < 4; ++k) { xn[i][k] = ((f32x2){blo(xv[i][k]), bhi(xv[i][k])} - m) * rs; asm volatile("" : "+v"(xn[i][k])); }
        RS_(12);
        { u32x4 a[2], b[2];
#pragma unroll 1
          for (u32 sp = 0; sp < SPIN_CAP; ++sp) { CFENCE();
              a[0] = ld4(r, C_MODP + tid * 32, 0); a[1] = ld4(r, C_MODP + tid * 32 + 16, 0); b[0] = ld4(r, C_MODP + 8192 + tid * 32, 0); b[1] = ld4(r, C_MODP + 8192 + tid * 32 + 16, 0);
              const u32 bad = (a[0].y ^ tag) | (a[0].w ^ tag) | (a[1].y ^ tag) | (a[1].w ^ tag) | (b[0].y ^ tag) | (b[0].w ^ tag) | (b[1].y ^ tag) | (b[1].w ^ tag);
              if (__all(bad == 0)) break;
              __builtin_amdgcn_s_sleep(1); }
          const f32x4 c = asf4((u32x4){a[0].x, a[0].z, a[1].x, a[1].z}) + asf4((u32x4){b[0].x, b[0].z, b[1].x, b[1].z}); const float o = tid < 128 ? 0.f : 1.f;
          *(f32x4*)(ms + tid * 4) = (f32x4){rbf(c[0]) + o, rbf(c[1]) + o, rbf(c[2]) + o, rbf(c[3]) + o}; }
        LBAR();
        RS_(13);
        u32x4 ho[8];
#pragma unroll
        for (int i = 0; i < 8; ++i) { const int c = i * 8 + cg;
#pragma unroll
            for (int k = 0; k < 2; ++k) { const f32x4 sv = *(const f32x4*)(ms + c * 8 + k * 4), cv = *(const f32x4*)(ms + 512 + c * 8 + k * 4);
                const f32x2 y0 = xn[i][2 * k] * (f32x2){cv[0], cv[1]} + (f32x2){sv[0], sv[1]}, y1 = xn[i][2 * k + 1] * (f32x2){cv[2], cv[3]} + (f32x2){sv[2], sv[3]};
                ho[i][2 * k] = pk2(y0[0], y0[1]); ho[i][2 * k + 1] = pk2(y1[0], y1[1]); } }
#pragma unroll
        for (int i = 0; i < 8; ++i) st4(r, ho[i], WS_H + sg * 32768 + ((i * 8 + cg) * 32 + tk) * 16, 0);
        DRAIN();
        if (lane == 0) st2(r, tag, ~tag, C_HF + sg * 128 + w * 8);
        RS_(14);
        if (w == 0) wait_init(r, tag, lane);
        if (sg >= 48 && w == 0) calib(p, r, sg - 48, tag);
        LBAR();
        mod_part<512, true>(p, r, lds, 1024 + sg * 32, 0, C_MOD2 + sg * 128, tag, []() {});
}
struct Touch { int next, ok; u32 off, p1; unsigned long long t0; };
DI void touch_step(rsrc_t r, Touch& t, int lane) {
    if (t.next > 63) return;
    if ((int)(__builtin_amdgcn_s_memrealtime() - t.t0) < (t.next - 6) * 170) return;
    if (t.next >= t.ok) { const u32x4 x = ld4(r, t.p1, 0); t.ok = __builtin_amdgcn_readfirstlane(min(min((int)x.x, (int)x.y), min((int)x.z, (int)x.w))) + 1; if (t.next >= t.ok) return; }
    const u32 y = ld1(r, t.off + lane * 128, KVS(t.next) * 16384); asm volatile("" :: "v"(y));
    ++t.next;
}
DI void wait_touch(rsrc_t r, u32 off, int want, Touch& t, int lane, bool quick) {
#pragma unroll 1
    for (u32 sp = 0; sp < SPIN_CAP; ++sp) {
        CFENCE();
        touch_step(r, t, lane);
        if (__all((int)ld1(r, off, 0) >= want)) break;
        if (quick) __builtin_amdgcn_s_sleep(2); else __builtin_amdgcn_s_sleep(ZSLEEP);
    }
    CFENCE();
}

DI void x_role(const Params& p, rsrc_t r, unsigned char* lds, int xi, const int tid) {
    const int lane = tid & 63, w = __builtin_amdgcn_readfirstlane(tid >> 6), g = lane >> 4, n = lane & 15;
    const int hp = xi & 15, chf = (xi >> 4) & 1, ph = xi < 64 ? xi >> 5 : 63, send = xi < 64 ? 63 : 64;
    const u32 pxo = WS_PX + hp * 32768 + (chf * 8 + w * 2) * 1024 + (n * 4 + g) * 16;
    for (int sg = ph; sg < send; sg += XPH) if (sg >= FAST0) {
#pragma unroll
        for (int i = 0; i < 4; ++i) st4(r, ALLFF, pxo + (i >> 1) * 16384 + (i & 1) * 1024, PXS(sg) * 524288); }
#pragma unroll
    for (int i = 0; i < 32; ++i) { const u32 lin = (i * 4 + w) * 4 + (lane >> 4), b = lin >> 8, row = lin & 255, ch = (lane & 15) ^ swz(row);
        __builtin_amdgcn_global_load_lds((const GAS u32*)(p.Wo + (size_t)(hp * 256 + row) * 512 + chf * 256 + b * 128 + ch * 8), (LAS u32*)(lds + (i * 4 + w) * 1024), 16, 0, 0);
        if (xi >= 64) __builtin_amdgcn_s_sleep(PACE); }
    DRAIN_DMA(); LBAR();
    const unsigned char* wl = lds + w * 32768 + lane * 16;
    { bf16x8 W0[8][4];
#pragma unroll
      for (int b = 0; b < 2; ++b) { const unsigned char* wb = lds + (w >> 1) * 65536 + imgoff(4 * g + (n >> 2), (w & 1) * 8 + b * 4 + (n & 3));
#pragma unroll
        for (int m = 0; m < 8; ++m)
#pragma unroll
            for (int t = 0; t < 2; ++t) W0[m][2 * b + t] = trfrag(wb + m * 8192 + 8 * t, wb + m * 8192 + 4096 + 8 * t); }
      LBAR();
#pragma unroll
      for (int m = 0; m < 8; ++m)
#pragma unroll
        for (int t = 0; t < 4; ++t) *(bf16x8*)(wl + (m * 4 + t) * 1024) = W0[m][t]; }
    const int h = hp * 2 + (w >> 1), qa = w & 1;
    const u32 p1 = C_P1 + h * 128;
    int known1 = wait_min6(r, p1, ph);
    u32 ko[2];
#pragma unroll
    for (int a = 0; a < 2; ++a) { const int t = 16 * a + n; ko[a] = WS_KV + h * RKV * 16384 + ((t >> 2) & 1) * 4096 + (g * 16 + (t >> 4) * 8 + ((t >> 3) & 1) * 4 + (t & 3)) * 16; }
    const u32 qo = WS_Q + h * RKV * 8192 + qa * 4096 + lane * 16, vo = WS_KV + h * RKV * 16384 + 8192 + lane * 16;
    const u32 kmo = WS_KM + h * 65 * 4096 + ((n >> 2) * 16 + 4 * g) * 8, vmo = WS_KM + h * 65 * 4096 + 2048 + lane * 8;
    const u32 kodd = 0u - (u32)(n & 1);
    const float cs = 0.08838834764831845f * 1.4426950408889634f;
    const int qi = 16 * qa + n;
    u32x4 qf[4], kf[2][4], vf[8];
#pragma unroll
    for (int k = 0; k < 4; ++k) { qf[k] = ld4(r, qo + k * 1024, KVS(ph) * 8192); kf[0][k] = ld4(r, ko[0] + k * 1024, KVS(ph) * 16384); kf[1][k] = ld4(r, ko[1] + k * 1024, KVS(ph) * 16384); }
#pragma unroll
    for (int t = 0; t < 8; ++t) vf[t] = ld4(r, vo + t * 1024, KVS(ph) * 16384);
    SCHED();
    int known = -1;
#pragma unroll 1
    for (int seg = ph; seg < send; seg += XPH) {
        f32x4 s0 = {0, 0, 0, 0}, s1 = {0, 0, 0, 0}, sm = {0, 0, 0, 0};
#pragma unroll
        for (int k = 0; k < 4; ++k) { s0 = MFMA(asf(kf[0][k]), asf(qf[k]), s0); s1 = MFMA(asf(kf[1][k]), asf(qf[k]), s1); }
        float mx = -1e30f;
#pragma unroll
        for (int i = 0; i < 4; ++i) { if (4 * g + i > qi) s0[i] = -1e30f; if (16 + 4 * g + i > qi) s1[i] = -1e30f; mx = fmaxf(mx, fmaxf(s0[i], s1[i])); }
        SCHED();
        if (known < seg) known = wait_min2(r, C_CP + (h * 2 + (lane & 1)) * 128, seg);
        if (seg >= 61) RS_(8);
        u32x4 ka[4], kb[4]; u32x2 vm[4];
#pragma unroll 1
        for (u32 sp = 0; sp < SPIN_CAP; ++sp) {
            CFENCE();
#pragma unroll
            for (int k = 0; k < 4; ++k) { ka[k] = ld4(r, kmo + k * 512, seg * 4096); kb[k] = ld4(r, kmo + k * 512 + 16, seg * 4096); vm[k] = ld2(r, vmo + k * 512, seg * 4096); }
            SCHED();
            if (seg < 63) break;
            u32 m = 0;
#pragma unroll
            for (int k = 0; k < 4; ++k) m = max(mx4(mx4(m, ka[k]), kb[k]), max(vm[k].x, vm[k].y));
            if (!__any(m == 0xFFFFFFFFu)) break;
        }
#pragma unroll
        for (int k = 0; k < 4; ++k) { const u32 a0 = ka[k].x, a1 = ka[k].y, a2 = ka[k].z, a3 = ka[k].w, b0 = kb[k].x, b1 = kb[k].y, b2 = kb[k].z, b3 = kb[k].w;
            const u32x4 m = {(a0 & ~kodd) | (a1 & kodd), (a2 & ~kodd) | (a3 & kodd), (b0 & ~kodd) | (b1 & kodd), (b2 & ~kodd) | (b3 & kodd)}; sm = MFMA(asf(m), asf(qf[k]), sm); }
        mx = fmaxf(mx, fmaxf(sm[0], sm[1]));
        mx = swap32_max(swap16_max(mx));
        const float mc = mx * cs;
        u32x4 px, pm;
        px[0] = pk2(__builtin_amdgcn_exp2f(s0[0] * cs - mc), __builtin_amdgcn_exp2f(s0[1] * cs - mc)); px[1] = pk2(__builtin_amdgcn_exp2f(s0[2] * cs - mc), __builtin_amdgcn_exp2f(s0[3] * cs - mc));
        px[2] = pk2(__builtin_amdgcn_exp2f(s1[0] * cs - mc), __builtin_amdgcn_exp2f(s1[1] * cs - mc)); px[3] = pk2(__builtin_amdgcn_exp2f(s1[2] * cs - mc), __builtin_amdgcn_exp2f(s1[3] * cs - mc));
        pm[0] = pk2(__builtin_amdgcn_exp2f(sm[0] * cs - mc), __builtin_amdgcn_exp2f(sm[1] * cs - mc)); pm[1] = 0; pm[2] = 0; pm[3] = 0;
        float su = blo(pm[0]) + bhi(pm[0]);
#pragma unroll
        for (int i = 0; i < 4; ++i) su += blo(px[i]) + bhi(px[i]);
        su = swap32_add(swap16_add(su));
        const float rs = __builtin_amdgcn_rcpf(su);
        if (seg == 63) RS_(9);
        unsigned char* ob = lds + 131072;
        f32x4 oo[8];
#pragma unroll
        for (int t = 0; t < 8; ++t) oo[t] = MFMA(asf(vf[t]), asf(px), ((f32x4){0, 0, 0, 0}));
#pragma unroll
        for (int t = 0; t < 8; ++t) { const u32x4 va = {vm[t >> 1][t & 1], 0u, 0u, 0u}; oo[t] = MFMA(asf(va), asf(pm), oo[t]); }
        SCHED();
#pragma unroll
        for (int m = 0; m < 4; ++m) { const f32x4 o[2] = {oo[2 * m], oo[2 * m + 1]};
            const u32x4 of = {pk2(o[0][0] * rs, o[0][1] * rs), pk2(o[0][2] * rs, o[0][3] * rs), pk2(o[1][0] * rs, o[1][1] * rs), pk2(o[1][2] * rs, o[1][3] * rs)};
            *(u32x4*)(ob + ((qa * 8 + (w >> 1) * 4 + m) * 64 + lane) * 16) = of; }
        bf16x8 W[8][4];
#pragma unroll
        for (int m = 0; m < 2; ++m)
#pragma unroll
            for (int t = 0; t < 4; ++t) W[m][t] = *(const bf16x8*)(wl + (m * 4 + t) * 1024);
        LBAR();
        f32x4 acc[2][4];
#pragma unroll
        for (int a = 0; a < 2; ++a)
#pragma unroll
            for (int t = 0; t < 4; ++t) acc[a][t] = (f32x4){0, 0, 0, 0};
        bf16x8 of[8][2];
#pragma unroll
        for (int m = 0; m < 2; ++m)
#pragma unroll
            for (int a = 0; a < 2; ++a) of[m][a] = *(const bf16x8*)(ob + ((a * 8 + m) * 64 + lane) * 16);
        SCHED();
#pragma unroll
        for (int m = 0; m < 8; ++m) {
            if (m + 2 < 8) {
#pragma unroll
                for (int t = 0; t < 4; ++t) W[m + 2][t] = *(const bf16x8*)(wl + ((m + 2) * 4 + t) * 1024);
#pragma unroll
                for (int a = 0; a < 2; ++a) of[m + 2][a] = *(const bf16x8*)(ob + ((a * 8 + m + 2) * 64 + lane) * 16); }
            if (m == XHINT && seg >= FAST0 && tid == 0) st1(r, seg, C_FX + xi * 128, 0);
            SCHED();
#pragma unroll
            for (int a = 0; a < 2; ++a)
#pragma unroll
                for (int t = 0; t < 4; ++t) acc[a][t] = MFMA(W[m][t], of[m][a], acc[a][t]);
            SCHED();
        }
#pragma unroll
        for (int a = 0; a < 2; ++a)
#pragma unroll
            for (int b = 0; b < 2; ++b) { const f32x4 x = acc[a][2 * b], y = acc[a][2 * b + 1]; const u32x4 o = {pk2(x[0], x[1]), pk2(x[2], x[3]), pk2(y[0], y[1]), pk2(y[2], y[3])};
                st4(r, guard(o), pxo + a * 16384 + b * 1024, PXS(seg) * 524288); }
        SCHED();
        if (seg == 63) RS_(6);
        if (seg + XPH < send) { const int nx = seg + XPH;
          if (known1 < nx) known1 = wait_min6(r, p1, nx);
#pragma unroll
          for (int k = 0; k < 4; ++k) { qf[k] = ld4(r, qo + k * 1024, KVS(nx) * 8192); kf[0][k] = ld4(r, ko[0] + k * 1024, KVS(nx) * 16384); kf[1][k] = ld4(r, ko[1] + k * 1024, KVS(nx) * 16384); }
#pragma unroll
          for (int t = 0; t < 8; ++t) vf[t] = ld4(r, vo + t * 1024, KVS(nx) * 16384); }
        SCHED();
        if (seg < FAST0) asm volatile("s_waitcnt vmcnt(20)" ::: "memory");
        LBAR();
        if (seg < FAST0 && tid == 0) st1(r, seg, C_FX + xi * 128, 0);
    }
}

DI void y_role(const Params& p, rsrc_t r, int yi, const int tid) {
    const int lane = tid & 63, w = __builtin_amdgcn_readfirstlane(tid >> 6);
    const int tk = (yi & 7) * 4 + w, ph = yi < 16 ? yi >> 3 : 60 + (yi >> 3), send = yi < 16 ? 62 : ph + 1;
    for (int sg = ph; sg < send; sg += 2) if (sg >= FAST0) { st4(r, ALLFF, WS_X1 + lane * 16, (sg * 32 + tk) * 1024); st4(r, ALLFF, WS_H2 + lane * 16, (sg * 32 + tk) * 1024); }
    DRAIN();
    float ga[8], sb[8], cb[8];
#pragma unroll
    for (int k = 0; k < 2; ++k) { const f32x4 a = asf4(get4(r, C_MOD2 + lane * 32 + k * 16)), b = asf4(get4(r, C_MOD2 + 2048 + lane * 32 + k * 16)), c = asf4(get4(r, C_MOD2 + 4096 + lane * 32 + k * 16));
#pragma unroll
        for (int j = 0; j < 4; ++j) { ga[k * 4 + j] = a[j]; sb[k * 4 + j] = b[j]; cb[k * 4 + j] = 1.f + c[j]; } }
#pragma unroll 1
    for (int seg = ph; seg < send; seg += 2) {
        const int row = seg * 32 + tk;
        const u32x4 xv = *(const u32x4*)(p.x + (size_t)row * 512 + lane * 8);
        if (w == 0) wait_ge(r, C_FX + ((seg == 63 ? 2 : seg & 1) * 32 + (lane & 31)) * 128, seg, 2);
        LBAR();
        if (seg == 63) RS_(8);
        float f[8];
#pragma unroll 1
        for (u32 sp = 0; sp < SPIN_CAP; ++sp) {
            CFENCE();
            u32x4 pv[16];
#pragma unroll
            for (int i = 0; i < 16; ++i) pv[i] = ld4(r, WS_PX + i * 32768 + ((tk >> 4) * 16 + (lane >> 2)) * 1024 + ((tk & 15) * 4 + (lane & 3)) * 16, PXS(seg) * 524288);
            SCHED();
            u32 m = 0;
            f32x2 a2[4] = {{0, 0}, {0, 0}, {0, 0}, {0, 0}};
#pragma unroll
            for (int i = 0; i < 16; ++i) { m = mx4(m, pv[i]);
#pragma unroll
                for (int j = 0; j < 4; ++j) a2[j] += (f32x2){blo(pv[i][j]), bhi(pv[i][j])}; }
#pragma unroll
            for (int j = 0; j < 4; ++j) { f[2 * j] = a2[j][0]; f[2 * j + 1] = a2[j][1]; }
            if (!__any(m == 0xFFFFFFFFu)) break;
        }
        u32x4 x1;
#pragma unroll
        for (int j = 0; j < 4; ++j) { x1[j] = pk2(blo(xv[j]) + ga[2 * j] * f[2 * j], bhi(xv[j]) + ga[2 * j + 1] * f[2 * j + 1]); f[2 * j] = blo(x1[j]); f[2 * j + 1] = bhi(x1[j]); }
        if (seg == 63) RS_(9);
        float s = 0;
#pragma unroll
        for (int j = 0; j < 8; ++j) s += f[j];
        float m = wsum(s) * (1.f / 512), q = 0;
#pragma unroll
        for (int j = 0; j < 8; ++j) { f[j] -= m; q += f[j] * f[j]; }
        float rs = __builtin_amdgcn_rsqf(wsum(q) * (1.f / 512) + 1e-5f);
        if (seg >= FAST0 && tid == 0) st1(r, seg, C_FY + yi * 128, 0);
        s = 0;
#pragma unroll
        for (int j = 0; j < 8; ++j) { f[j] = f[j] * rs * cb[j] + sb[j]; s += f[j]; }
        m = wsum(s) * (1.f / 512); q = 0;
#pragma unroll
        for (int j = 0; j < 8; ++j) { f[j] -= m; q += f[j] * f[j]; }
        rs = __builtin_amdgcn_rsqf(wsum(q) * (1.f / 512) + 1e-5f);
        const u32x4 o = {pk2(f[0] * rs, f[1] * rs), pk2(f[2] * rs, f[3] * rs), pk2(f[4] * rs, f[5] * rs), pk2(f[6] * rs, f[7] * rs)};
        st4(r, guard(o), WS_H2 + lane * 16, row * 1024);
        st4(r, guard(x1), WS_X1 + lane * 16, row * 1024);
        if (seg == 63) RS_(6);
        if (seg < FAST0) { DRAIN(); LBAR();
            if (tid == 0) st1(r, seg, C_FY + yi * 128, 0); }
    }
}

DI void z_role(const Params& p, rsrc_t r, unsigned char* lds, int zi, const int tid, Touch& tc) {
    const int lane = tid & 63, w = __builtin_amdgcn_readfirstlane(tid >> 6), g = lane >> 4, n = lane & 15;
    const int dt = zi & 3, ft = zi >> 2, wf = w >> 1, wd = w & 1;
    f32x4 acc[4][4];
#pragma unroll
    for (int i = 0; i < 4; ++i) { const u32x2 bv = *(const u32x2*)(p.b1 + ft * 128 + wf * 64 + i * 16 + 4 * g);
#pragma unroll
        for (int j = 0; j < 4; ++j) acc[i][j] = (f32x4){blo(bv[0]), bhi(bv[0]), blo(bv[1]), bhi(bv[1])}; }
    const u32 uo = WS_U + ((dt * 8 + wd * 4) * 56 + ft * 4 + wf * 2) * 1024 + lane * 16;
#pragma unroll
    for (int i = 0; i < 8; ++i) st4(r, ALLFF, uo + (i >> 1) * 57344 + (i & 1) * 1024, 0);
    DRAIN();
    const int row = tid >> 4, ch = tid & 15;
    const u32 lo = imgoff(row, ch);
    const u16* wp = p.w1 + (size_t)row * F_ + ft * 128 + ch * 8;
    const u32 ho = WS_H2 + row * 1024 + dt * 256 + ch * 16;
    const int q = n >> 2, pp = n & 3;
    u32 ra[4][2], rb[4][2];
#pragma unroll
    for (int i = 0; i < 4; ++i)
#pragma unroll
        for (int k = 0; k < 2; ++k) { ra[i][k] = imgoff(8 * g + 4 * k + q, (wf * 4 + i) * 2 + (pp >> 1)) + 8 * (pp & 1); rb[i][k] = 16384 + imgoff(8 * g + 4 * k + q, (wd * 4 + i) * 2 + (pp >> 1)) + 8 * (pp & 1); }
    u32x4 wv[4];
#pragma unroll
    for (int i = 0; i < 4; ++i) wv[i] = *(const u32x4*)(wp + (size_t)i * 16 * F_);
#pragma unroll 1
    for (int pr = 0; pr < 32; ++pr) {
        unsigned char* L = lds + (pr & 1) * 32768;
#pragma unroll
        for (int i = 0; i < 4; ++i) *(u32x4*)(L + i * 4096 + lo) = wv[i];
        if (pr < 31) {
#pragma unroll
            for (int i = 0; i < 4; ++i) wv[i] = *(const u32x4*)(wp + (size_t)((pr + 1) * 64 + i * 16) * F_); }
        if (w == 0) wait_touch(r, C_FY + ((pr == 31 ? 16 : 0) + (lane & 15)) * 128, 2 * pr + ((lane >> 3) & 1), tc, lane, 2 * pr >= FAST0);
        LBAR();
        if (pr == 31) RS_(8);
        if (pr == 30) RS_(6);
        u32x4 hv[4];
#pragma unroll 1
        for (u32 sp = 0; sp < SPIN_CAP; ++sp) {
            CFENCE();
#pragma unroll
            for (int i = 0; i < 4; ++i) hv[i] = ld4(r, ho + i * 16384, pr * 65536);
            if (!__any(mx4(mx4(mx4(mx4(0u, hv[0]), hv[1]), hv[2]), hv[3]) == 0xFFFFFFFFu)) break;
        }
#pragma unroll
        for (int i = 0; i < 4; ++i) *(u32x4*)(L + 16384 + i * 4096 + lo) = hv[i];
        bf16x8 fa[2][4], fb[2][4];
#pragma unroll
        for (int k = 0; k < 2; ++k)
#pragma unroll
            for (int i = 0; i < 4; ++i) fa[k][i] = trfrag(L + k * 8192 + ra[i][0], L + k * 8192 + ra[i][1]);
        LBAR();
#pragma unroll
        for (int k = 0; k < 2; ++k)
#pragma unroll
            for (int i = 0; i < 4; ++i) fb[k][i] = trfrag(L + k * 8192 + rb[i][0], L + k * 8192 + rb[i][1]);
        SCHED();
#pragma unroll
        for (int k = 0; k < 2; ++k)
#pragma unroll
            for (int j = 0; j < 4; ++j)
#pragma unroll
                for (int i = 0; i < 4; ++i) acc[i][j] = MFMA(fa[k][i], fb[k][j], acc[i][j]);
    }
    RS_(9);
#pragma unroll
    for (int k = 0; k < 2; ++k) {
#pragma unroll
        for (int j = 0; j < 4; ++j) { u32x4 o;
            if (k * 4 + j == ZHINT && tid == 0) st1(r, 0, C_FU + (dt * 14 + ft) * 128, 0);
#pragma unroll
            for (int t = 0; t < 4; ++t) { const f32x2 v = {acc[2 * k + (t >> 1)][j][(t & 1) * 2], acc[2 * k + (t >> 1)][j][(t & 1) * 2 + 1]};
                const f32x2 zz = v * (v * v * -0.1029432397f - 2.302208198f);
                const f32x2 e = (f32x2){__builtin_amdgcn_exp2f(zz[0]), __builtin_amdgcn_exp2f(zz[1])} + 1.f;
                const f32x2 y = v * (f32x2){__builtin_amdgcn_rcpf(e[0]), __builtin_amdgcn_rcpf(e[1])};
                o[t] = pk2(y[0], y[1]); }
            st4(r, guard(o), uo + j * 57344 + k * 1024, 0);
            SCHED(); }
    }
}

#define G2D 10
#define G2MAP 2
DI void g2_tile(int& st, int& dt) {
    const int xc = blockIdx.x & 7, sl = blockIdx.x >> 3;
    if (xc >= 4 || G2MAP == 0) { st = sl; dt = xc; }
    else if (G2MAP == 1) { dt = (xc & 2) + (sl & 1); st = (xc & 1) * 16 + (sl >> 1); }
    else { dt = sl & 3; st = xc * 8 + (sl >> 2); }
}
DI void g2_load(const Params& p, unsigned char* lds, bf16x8 (&wf)[14][4], const int tid, const bool pace) {
    const int lane = tid & 63, w = __builtin_amdgcn_readfirstlane(tid >> 6), g = lane >> 4, n = lane & 15;
    int st, dt; g2_tile(st, dt);
    unsigned char* L = lds + w * 28672;
    const int q = n >> 2, pp = n & 3;
    LBAR();
#pragma unroll
    for (int hf = 0; hf < 2; ++hf) {
#pragma unroll 4
        for (int i = 0; i < 28; ++i) { const u32 row = i * 8 + (lane >> 3), ch = (lane & 7) ^ (((row >> 1) & 3) << 1);
            __builtin_amdgcn_global_load_lds((const GAS u32*)(p.w2 + (size_t)(w * 448 + hf * 224 + row) * S_ + st * 64 + ch * 8), (LAS u32*)(L + i * 1024), 16, 0, 0);
            if (pace) __builtin_amdgcn_s_sleep(PACE); }
        DRAIN_DMA();
#pragma unroll
        for (int k = 0; k < 7; ++k)
#pragma unroll
            for (int j = 0; j < 4; ++j) { const u32 r0 = 4 * g + q, o = r0 * 128 + 16 * ((j * 2 + (pp >> 1)) ^ (((r0 >> 1) & 3) << 1)) + 8 * (pp & 1);
                wf[hf * 7 + k][j] = trfrag(L + k * 4096 + o, L + k * 4096 + 2048 + o); }
#pragma unroll
        for (int k = 0; k < 7; ++k)
#pragma unroll
            for (int j = 0; j < 4; ++j) if (k < 6) PIN_A(wf[hf * 7 + k][j]);
        asm volatile("s_waitcnt lgkmcnt(0)" ::: "memory");
    }
    LBAR();
}
DI void g2_finish(const Params& p, rsrc_t r, unsigned char* lds, const bf16x8 (&wf)[14][4], const int tid) {
    const int lane = tid & 63, w = __builtin_amdgcn_readfirstlane(tid >> 6), g = lane >> 4, n = lane & 15;
    int st, dt; g2_tile(st, dt);
    const int c = (tid & 7) * 8, col = dt * 64 + c;
    const f32x4 g0 = asf4(get4(r, C_MOD2 + 6144 + col * 4)), g1 = asf4(get4(r, C_MOD2 + 6144 + col * 4 + 16));
    const float b2a = bf2f(p.b2[st * 64 + (tid >> 3)]), b2b = bf2f(p.b2[st * 64 + 32 + (tid >> 3)]);
    if (w == 0) { wait_ge(r, C_FX + 64 * 128, 63, 16); wait_ge(r, C_FU + ((dt >> 1) * 14 + lane % 14) * 128, 0, 2); }
    LBAR();
    RS_(10);
    const u32 uo = WS_U + ((dt * 4) * 56 + w * 14) * 1024 + lane * 16;
    u32 so[4];
#pragma unroll
    for (int i = 0; i < 4; ++i) so[i] = ((i + st) & 3) * 57344;
    f32x4 acc[4][4];
    u32x4 xa, xb;
#pragma unroll 1
    for (u32 sp = 0; sp < SPIN_CAP; ++sp) {
        CFENCE();
#pragma unroll
        for (int i = 0; i < 4; ++i)
#pragma unroll
            for (int j = 0; j < 4; ++j) acc[i][j] = (f32x4){0, 0, 0, 0};
        u32x4 ua[56]; u32 m = 0;
#pragma unroll
        for (int q = 0; q < G2D; ++q) ua[q] = ld4(r, uo + (q % 14) * 1024, so[q / 14]);
        SCHED();
#pragma unroll
        for (int q = 0; q < 56; ++q) {
            if (q + G2D < 56) ua[q + G2D] = ld4(r, uo + ((q + G2D) % 14) * 1024, so[(q + G2D) / 14]);
            if (q + G2D == 56) { xa = ld4(r, WS_X1 + (st * 64 + (tid >> 3)) * 1024 + col * 2, 0); xb = ld4(r, WS_X1 + (st * 64 + 32 + (tid >> 3)) * 1024 + col * 2, 0); }
            SCHED();
#pragma unroll
            for (int j = 0; j < 4; ++j) acc[q / 14][j] = MFMA(asf(ua[q]), wf[q % 14][j], acc[q / 14][j]);
            m = mx4(m, ua[q]);
            SCHED();
        }
        if (!__any(mx4(mx4(m, xa), xb) == 0xFFFFFFFFu)) break;
    }
    RS_(11);
    float* red = (float*)lds;
#pragma unroll
    for (int i = 0; i < 4; ++i)
#pragma unroll
        for (int j = 0; j < 4; ++j) *(f32x4*)(red + (w * 64 + 16 * j + n) * 68 + 16 * ((i + st) & 3) + 4 * g) = acc[i][j];
    LBAR();
#pragma unroll
    for (int k = 0; k < 2; ++k) { const int s = (tid >> 3) + 32 * k; f32x4 a = {0, 0, 0, 0}, bq = {0, 0, 0, 0};
#pragma unroll
        for (int ww = 0; ww < 4; ++ww) { a += *(const f32x4*)(red + (ww * 64 + s) * 68 + c); bq += *(const f32x4*)(red + (ww * 64 + s) * 68 + c + 4); }
        const float b2 = k ? b2b : b2a; const u32x4 x1 = k ? xb : xa;
        const u32x4 o = {pk2(blo(x1[0]) + g0[0] * (a[0] + b2), bhi(x1[0]) + g0[1] * (a[1] + b2)), pk2(blo(x1[1]) + g0[2] * (a[2] + b2), bhi(x1[1]) + g0[3] * (a[3] + b2)),
                         pk2(blo(x1[2]) + g1[0] * (bq[0] + b2), bhi(x1[2]) + g1[1] * (bq[1] + b2)), pk2(blo(x1[3]) + g1[2] * (bq[2] + b2), bhi(x1[3]) + g1[3] * (bq[3] + b2))};
        *(u32x4*)(p.out + (size_t)(st * 64 + s) * 512 + col) = o; }
}

__global__ void __launch_bounds__(256, 1) k_main(Params p0) {
    extern __shared__ __attribute__((aligned(16))) unsigned char lds[];
    asm volatile("" :: "s"(p0.ws));
    const int b = blockIdx.x, xc = b & 7, sl = b >> 3;
    int tid = threadIdx.x;
    const int wv = __builtin_amdgcn_readfirstlane(tid >> 6);
    { const Params& p = p0; RS_(0); }
    const bool chain = xc < 4 && sl < 16;
    const int j = xc >= 4 ? sl * 4 + xc - 4 : 128 + (sl - 16) * 4 + xc;
    if (chain) chain_role(p0, lds, xc * 16 + sl);
    else { const Params& p = p0; const rsrc_t r = __builtin_amdgcn_make_buffer_rsrc(p.ws, 0, 256u * MB, 0x00020000);
        const u32 tag = ld1(r, WS_EP, 0) + 1u;
        if (j < 48) { proj_role(p, r, lds, j, tag); RS_(1); }
        else if (j >= 128) ln_role(p, r, lds, j - 128, tag);
        else if (j >= 112) {
            for (u32 o = (j - 112) * 4096 + tid * 16; o < CTL_BYTES + 16384; o += 65536) st4(r, ALLFF, o < 16384 ? WS_MAIL + o : WS_CTL + o - 16384, 0);
            DRAIN(); LBAR();
            if (tid == 0) st2(r, tag, ~tag, WS_DN + (j - 112) * 128);
            __builtin_amdgcn_s_sleep(64);
            if (wv == 0) wait_init(r, tag, tid & 63);
        } else {
            __builtin_amdgcn_s_sleep(100);
#pragma unroll
            for (int k = 0; k < 8; ++k) { const u32 o = (j - 48) * 32768 + k * 4096 + tid * 16; if (o >= 16384) st4(r, ALLFF, WS_MAIL + o, 0); }
            DRAIN(); LBAR();
            if (wv == 0) wait_init(r, tag, tid & 63);
        { const int i = j - 48;
    if (tid < 64) {
        const int u = (tid & 31) * 2; const int taup = u & 3, pb = (u >> 2) & 1, tk = (u >> 3) & 1, pr = (u >> 4) & 1, gal = (u >> 5) & 1, m = i >> 2, gp = i & 3;
        const int d = 64 * (m >> 1) + 16 * taup + 8 * (m & 1) + 2 * (2 * gal + pr) + pb;
        const u32 val = (u32)p.mem0[(2 * gp + tk) * 512 + d] | ((u32)p.mem0[(2 * gp + tk) * 512 + d + 16] << 16);
        __hip_atomic_store((GAS u32*)(p.ws + WS_MAIL + (tid >> 5) * 8192 + i * 128 + (tid & 31) * 4), min(val, 0xFFFFFFFEu), RLX_AGENT);
    }
        }
        }
    }
    const __attribute__((address_space(4))) Params* pp = (const __attribute__((address_space(4))) Params*)__builtin_amdgcn_kernarg_segment_ptr();
    asm volatile("" : "+s"(pp));
    Params p; p.x = pp->x; p.Wo = pp->Wo; p.w1 = pp->w1; p.b1 = pp->b1; p.w2 = pp->w2; p.b2 = pp->b2; p.out = pp->out; p.ws = pp->ws;
    const rsrc_t r = __builtin_amdgcn_make_buffer_rsrc(p.ws, 0, 256u * MB, 0x00020000);
    asm volatile("v_mbcnt_lo_u32_b32 %0, -1, 0\n\tv_mbcnt_hi_u32_b32 %0, -1, %0\n\tv_lshl_add_u32 %0, %1, 6, %0" : "=&v"(tid) : "s"(wv));
    RS_(2);
    bf16x8 wf[14][4];
    Touch tc;
    { const int rk = (j & 3) * 16 + ((j & 63) >> 2); tc.next = 2; tc.ok = 0; tc.off = WS_KV + (rk >> 1) * RKV * 16384 + (rk & 1) * 8192; tc.p1 = C_P1 + (rk >> 1) * 128; tc.t0 = __builtin_amdgcn_s_memrealtime(); }
    if (!chain) {
        if (j >= 128) { if (wv == 0) wait_touch(r, C_CP + (j & 63) * 128, (j >> 2) & 7, tc, tid, false); }
        else { const unsigned long long t1 = tc.t0 + (j < 48 ? 0 : G2_T0) + ((j >> 2) & 7) * 50; while (__builtin_amdgcn_s_memrealtime() < t1) __builtin_amdgcn_s_sleep(8); }
    }
    g2_load(p, lds, wf, tid, !chain && (j < 48 || j >= 112));
    if (!chain) {
        if (j < 32 || (j >= 48 && j < 112)) x_role(p, r, lds, j < 32 ? 64 + j : j - 48, tid);
        else if (j < 128) y_role(p, r, j < 48 ? j - 16 : j - 112, tid);
        else if (j < 184) z_role(p, r, lds, j - 128, tid, tc);
        else if (wv == 0) wait_touch(r, C_FX + 64 * 128, 63, tc, tid, false);
    }
    RS_(5);
    g2_finish(p, r, lds, wf, tid);
    RS_(7);
    if (blockIdx.x == 0 && tid == 0) st1(r, ld1(r, WS_EP, 0) + 1u, WS_EP, 0);
}

extern "C" void kernel_launch(void* const* d_in, const int* in_sizes, int n_in, void* d_out, int out_size, void* d_ws, size_t ws_size, hipStream_t stream) {
    Params p;
    p.x = (const u16*)d_in[0]; p.mem0 = (const u16*)d_in[1]; p.Wq = (const u16*)d_in[2]; p.Wk = (const u16*)d_in[3]; p.Wv = (const u16*)d_in[4]; p.Wo = (const u16*)d_in[5];
    p.w1 = (const u16*)d_in[8]; p.b1 = (const u16*)d_in[9]; p.w2 = (const u16*)d_in[10]; p.b2 = (const u16*)d_in[11]; p.c = (const u16*)d_in[12]; p.wm = (const u16*)d_in[13];
    p.out = (u16*)d_out; p.ws = (unsigned char*)d_ws;
    (void)hipFuncSetAttribute((const void*)k_main, hipFuncAttributeMaxDynamicSharedMemorySize, L_TOTAL);
    k_main<<<256, 256, L_TOTAL, stream>>>(p);
}
```
